```python
import torch

def _softmax(x, axis=-1):
    return torch.softmax(x, dim=axis)
def _gelu(x):
    return torch.nn.functional.gelu(x, approximate="tanh")
def _silu(x):
    return torch.nn.functional.silu(x)
def _sigmoid(x):
    return torch.sigmoid(x)
def _layernorm(x, axis=-1, eps=1e-5):
    m = x.mean(dim=axis, keepdim=True)
    v = x.var(dim=axis, keepdim=True, unbiased=False)
    return (x - m) / torch.sqrt(v + eps)
def _scatter_add(tgt, idx, src, axis=0):
    return tgt.clone().index_add_(axis, idx, src)
def _segment_sum(src, seg, n):
    out = torch.zeros((n,) + src.shape[1:], dtype=src.dtype)
    return out.index_add_(0, seg, src)
def _pad(x, pw):
    flat = tuple(v for pair in reversed(pw) for v in pair)
    return torch.nn.functional.pad(x, flat)

kernel_name = 'interleave_layers__gqa_block_cached__mixer_token_mlp__decode'

def setup_inputs():

    _w = (lambda a: (lambda t: t.to(torch.bfloat16) if t.is_floating_point() else t)(torch.as_tensor(a)))
    def _A_setup():
        B, Sq, D = 8, 1, 448
        Hq, Hkv, Sk, Dh = 16, 8, 8192, 256
        assert Hq % Hkv == 0, (Hq, Hkv)
        return {
            "x": _w((lambda *s: torch.randn(*s, dtype=torch.bfloat16) * 0.05)(B, Sq, D)),
            "k_cache": _w((lambda *s: torch.randn(*s, dtype=torch.bfloat16) * 0.8)(B, Hkv, Sk, Dh)),
            "v_cache": _w((lambda *s: torch.randn(*s, dtype=torch.bfloat16) * 1.6)(B, Hkv, Sk, Dh)),
            "wq": _w((lambda *s: torch.randn(*s, dtype=torch.bfloat16) * 0.8)(D, Hq * Dh) * (D ** -0.5)),
            "wo": _w((lambda *s: torch.randn(*s, dtype=torch.bfloat16) * 1.6)(Hq * Dh, D) * ((Hq * Dh) ** -0.5)),
        }
    _A_in = _A_setup()

    def _B_setup():
        B, S, D, F = 8, 1, 448, 1792
        return {
            "x": _w((lambda *s: torch.randn(*s, dtype=torch.bfloat16) * 0.05)(B, S, D)),
            "w1": _w((lambda *s: torch.randn(*s, dtype=torch.bfloat16) * 0.8)(S, F) * (S ** -0.5)),
            "b1": _w((lambda *s: torch.randn(*s, dtype=torch.bfloat16) * 0.8)(F) * 0.02),
            "w2": _w((lambda *s: torch.randn(*s, dtype=torch.bfloat16) * 0.2)(F, S) * (F ** -0.5)),
            "b2": _w((lambda *s: torch.randn(*s, dtype=torch.bfloat16) * 0.2)(S) * 0.02),
        }
    _B_in = _B_setup()

    return {
        "A_x": _A_in["x"],
        "A_k_cache": _A_in["k_cache"],
        "A_v_cache": _A_in["v_cache"],
        "A_wq": _A_in["wq"],
        "A_wo": _A_in["wo"],
        "B_x": _B_in["x"],
        "B_w1": _B_in["w1"],
        "B_b1": _B_in["b1"],
        "B_w2": _B_in["w2"],
        "B_b2": _B_in["b2"],
    }

def reference(A_x, A_k_cache, A_v_cache, A_wq, A_wo, B_x, B_w1, B_b1, B_w2, B_b2):
    def _A_ref(x, k_cache, v_cache, wq, wo):
        B, Sq, D = x.shape
        Hq, Dh = 16, 256
        Hkv = k_cache.shape[1]
        rep = Hq // Hkv
        q = (lambda x, *d: x.permute(*d))((x @ wq).reshape(B, Sq, Hq, Dh), 0, 2, 1, 3)
        k = (lambda x, n, axis: torch.repeat_interleave(x, n, dim=axis))(k_cache, rep, axis=1)
        v = (lambda x, n, axis: torch.repeat_interleave(x, n, dim=axis))(v_cache, rep, axis=1)
        scores = torch.einsum("bhqd,bhkd->bhqk", q, k) * (Dh ** -0.5)
        p = _softmax(scores, axis=-1)
        o = torch.einsum("bhqk,bhkd->bhqd", p, v)
        return (lambda x, *d: x.permute(*d))(o, 0, 2, 1, 3).reshape(B, Sq, Hq * Dh) @ wo

    def _B_ref(x, w1, b1, w2, b2):
        B, S, D = x.shape
        h = _layernorm(x, axis=-1)
        u = torch.einsum("bsd,sf->bdf", h, w1) + b1
        u = _gelu(u)
        y = torch.einsum("bdf,fs->bds", u, w2) + b2
        return (lambda x, *d: x.permute(*d))(y, 0, 2, 1)

    x = A_x
    x = x + _A_ref(_layernorm(x, axis=-1), A_k_cache, A_v_cache, A_wq, A_wo)
    x = x + _B_ref(_layernorm(x, axis=-1), B_w1, B_b1, B_w2, B_b2)
    x = x + _A_ref(_layernorm(x, axis=-1), A_k_cache, A_v_cache, A_wq, A_wo)
    x = x + _B_ref(_layernorm(x, axis=-1), B_w1, B_b1, B_w2, B_b2)
    return x
```

```cpp
#include <hip/hip_runtime.h>
#include <hip/hip_bf16.h>
#include <stdint.h>

#define LAS __attribute__((address_space(3)))
typedef unsigned v4u __attribute__((ext_vector_type(4)));
typedef unsigned v2u __attribute__((ext_vector_type(2)));
typedef float f32x4 __attribute__((ext_vector_type(4)));
typedef __bf16 bf16x2_t __attribute__((ext_vector_type(2)));
typedef __bf16 bf16x8_t __attribute__((ext_vector_type(8)));
typedef __amdgpu_buffer_rsrc_t rsrc_t;

namespace {

constexpr int NB = 8, DM = 448, HKV = 8, SK = 8192, DH = 256, FF = 1792;
constexpr unsigned SENT = 0xFFFFFFFFu;
constexpr int AUX_NT = 2, AUX_SC1 = 16;
constexpr unsigned SPIN_CAP = 1u << 16;
constexpr float EPS = 1e-5f;
constexpr float SCL = 0.0625f * 1.4426950408889634f;

#define AUX_W 0
#define XD_REP 8
#define SLP_B 8
#define SLP_D 8
#define SLP_C 6
#define SLP_A 4
#define FIRST_POLL_TICKS 600
#define STAGGER_TICKS 1000

constexpr unsigned WS_EPOCH = 0;
constexpr unsigned WS_FLAGS = 1024;
constexpr unsigned WS_X0 = 4096;
constexpr unsigned WS_Q1 = 32768;
constexpr unsigned WS_S0 = 131072;
constexpr unsigned PSL = 1280;
constexpr unsigned WS_PART = WS_S0;
constexpr unsigned PART_BYTES = 8u * 2 * 8 * 16 * PSL;
constexpr unsigned WS_CB = WS_PART + PART_BYTES;
constexpr unsigned CBR = 8 * 1792;
constexpr unsigned WS_XD = WS_CB + 16 * CBR;
constexpr unsigned XD_BYTES = 8u * XD_REP * 1792;
constexpr unsigned WS_Q2 = WS_XD + XD_BYTES;
constexpr unsigned WS_SEND = WS_Q2 + 65536;
constexpr unsigned WS_STAMP = 8u << 20;
constexpr unsigned WS_BYTES = 16u << 20;
static_assert(WS_SEND <= WS_STAMP, "");
static_assert((WS_SEND - WS_S0) % 16 == 0, "");

constexpr unsigned L_K = 0;
constexpr unsigned L_MRG = 131072;
constexpr unsigned L_MST = L_MRG + 16384;
constexpr unsigned L_CS = L_MST + 128;
constexpr unsigned L_FR = L_CS + 128;
constexpr unsigned FR_RED1 = 0, FR_X1 = 1024, FR_ST = 1024 + 1792, FR_RED2 = FR_ST + 64, FR_ROW = FR_RED2 + 128;
constexpr unsigned L_BK = L_FR + FR_ROW;
constexpr unsigned BK_ROW = 1024 + 64;
constexpr unsigned L_Q = L_BK + BK_ROW, L_QF = L_Q + 1024;
constexpr unsigned L_END = L_QF + 16;
constexpr unsigned LDS_BYTES = 163840;
static_assert(L_END <= LDS_BYTES, "");
static_assert(FR_ROW % 16 == 0 && L_FR % 16 == 0 && L_BK % 16 == 0, "");

struct Params {
  const void* x; const void* k; const void* v; const void* wq; const void* wo;
  const void* w1; const void* b1; const void* w2; const void* b2;
  void* out; void* ws;
};

#define SCHED() __builtin_amdgcn_sched_barrier(0)
#define PIN2(a, b) asm volatile("" : "+v"(a), "+v"(b))
#define CFENCE() asm volatile("" ::: "memory")

__device__ __forceinline__ float asf(unsigned u) { return __builtin_bit_cast(float, u); }
__device__ __forceinline__ unsigned asu(float f) { return __builtin_bit_cast(unsigned, f); }
__device__ __forceinline__ float bflo(unsigned u) { return asf(u << 16); }
__device__ __forceinline__ float bfhi(unsigned u) { return asf(u & 0xffff0000u); }
__device__ __forceinline__ unsigned cvtpk(float lo, float hi) {
  bf16x2_t r; r[0] = (__bf16)lo; r[1] = (__bf16)hi; return __builtin_bit_cast(unsigned, r);
}
__device__ __forceinline__ float dot2(unsigned a, unsigned b, float c) {
  return __builtin_amdgcn_fdot2_f32_bf16(__builtin_bit_cast(bf16x2_t, a), __builtin_bit_cast(bf16x2_t, b), c, false);
}
template <int CTRL> __device__ __forceinline__ float dpp(float x) {
  return __builtin_bit_cast(float, __builtin_amdgcn_mov_dpp(__builtin_bit_cast(int, x), CTRL, 0xf, 0xf, true));
}
template <int CTRL> __device__ __forceinline__ unsigned dppu(unsigned x) {
  return (unsigned)__builtin_amdgcn_mov_dpp((int)x, CTRL, 0xf, 0xf, true);
}
constexpr int XOR1 = 0xB1, XOR2 = 0x4E, HMIR = 0x141, ROR8 = 0x128, ROR4 = 0x124;
__device__ __forceinline__ float sum8(float x) { x += dpp<HMIR>(x); x += dpp<XOR1>(x); x += dpp<XOR2>(x); return x; }
__device__ __forceinline__ float sum16(float x) { x += dpp<ROR8>(x); return sum8(x); }
__device__ __forceinline__ float sumx16(float x) {
  auto s = __builtin_amdgcn_permlane16_swap(asu(x), asu(x), false, false); return asf(s[0]) + asf(s[1]);
}
__device__ __forceinline__ float sumx32(float x) {
  auto s = __builtin_amdgcn_permlane32_swap(asu(x), asu(x), false, false); return asf(s[0]) + asf(s[1]);
}
__device__ __forceinline__ float maxx16(float x) {
  auto s = __builtin_amdgcn_permlane16_swap(asu(x), asu(x), false, false); return fmaxf(asf(s[0]), asf(s[1]));
}
__device__ __forceinline__ float maxx32(float x) {
  auto s = __builtin_amdgcn_permlane32_swap(asu(x), asu(x), false, false); return fmaxf(asf(s[0]), asf(s[1]));
}
__device__ __forceinline__ f32x4 mfma(v4u a, v4u b, f32x4 c) {
  return __builtin_amdgcn_mfma_f32_16x16x32_bf16(__builtin_bit_cast(bf16x8_t, a), __builtin_bit_cast(bf16x8_t, b), c, 0, 0, 0);
}
__device__ __forceinline__ unsigned umax4(v4u v) { return max(max(v[0], v[1]), max(v[2], v[3])); }
__device__ __forceinline__ unsigned nosent(unsigned u) { return min(u, 0xFFFFFFFEu); }
__device__ __forceinline__ v4u nosent4(f32x4 v) { return v4u{nosent(asu(v[0])), nosent(asu(v[1])), nosent(asu(v[2])), nosent(asu(v[3]))}; }
__device__ __forceinline__ void wg_bar() {
  asm volatile("s_waitcnt lgkmcnt(0)" ::: "memory");
  __builtin_amdgcn_s_barrier();
  CFENCE();
}
__device__ __forceinline__ int fresh(int x) { asm volatile("" : "+v"(x)); return x; }
__device__ __forceinline__ rsrc_t mkrs(const void* p, unsigned bytes) {
  return __builtin_amdgcn_make_buffer_rsrc((void*)p, 0, bytes, 0x00020000);
}
template <int AUX> __device__ __forceinline__ v4u ld16(rsrc_t r, unsigned vo, unsigned so) { return __builtin_amdgcn_raw_buffer_load_b128(r, vo, so, AUX); }
template <int AUX> __device__ __forceinline__ v2u ld8(rsrc_t r, unsigned vo, unsigned so) { return __builtin_amdgcn_raw_buffer_load_b64(r, vo, so, AUX); }
template <int AUX> __device__ __forceinline__ unsigned ld4(rsrc_t r, unsigned vo, unsigned so) { return __builtin_amdgcn_raw_buffer_load_b32(r, vo, so, AUX); }
__device__ __forceinline__ void st16(v4u v, rsrc_t r, unsigned vo, unsigned so) { __builtin_amdgcn_raw_buffer_store_b128(v, r, vo, so, AUX_SC1); }
__device__ __forceinline__ LAS f32x4* lf4(LAS char* L, unsigned off) { return (LAS f32x4*)(L + off); }
__device__ __forceinline__ LAS float* lf(LAS char* L, unsigned off) { return (LAS float*)(L + off); }

#define STAMP(i) do {} while (0)
#define STAMP_INIT() do {} while (0)
#define STAMP_DUMP() do {} while (0)

__device__ __forceinline__ unsigned long long mktag(v2u ep) {
  const unsigned long long e = ((unsigned long long)(unsigned)__builtin_amdgcn_readfirstlane(ep[1]) << 32) | (unsigned)__builtin_amdgcn_readfirstlane(ep[0]);
  return e * 0x9E3779B97F4A7C15ull + 0x632BE59BD9B4E019ull;
}
#define SKIP_BEGIN(gate, id, rs, vo) do { SCHED(); asm volatile("s_cmp_eq_u32 %0, 0\n\ts_cbranch_scc1 .Lfd_skip%c1" :: "s"(gate), "i"(id), "s"(rs), "v"(vo) : "scc"); SCHED(); } while (0)
#define SKIP_END(id) do { SCHED(); asm volatile(".Lfd_skip%c0:" :: "i"(id) : "scc"); SCHED(); } while (0)

__device__ __forceinline__ float tr32(float a, float b) { auto s = __builtin_amdgcn_permlane32_swap(asu(a), asu(b), false, false); return asf(s[0]) + asf(s[1]); }
__device__ __forceinline__ float tr16(float a, float b) { auto s = __builtin_amdgcn_permlane16_swap(asu(a), asu(b), false, false); return asf(s[0]) + asf(s[1]); }
template <int CTRL, int RM> __device__ __forceinline__ float dppz(float x) {
  return __builtin_bit_cast(float, __builtin_amdgcn_update_dpp(0, __builtin_bit_cast(int, x), CTRL, RM, 0xf, true));
}
__device__ __forceinline__ void wsum2(float& a, float& b) {
  a += dpp<ROR8>(a); b += dpp<ROR8>(b);
  a += dpp<HMIR>(a); b += dpp<HMIR>(b);
  a += dpp<XOR1>(a); b += dpp<XOR1>(b);
  a += dpp<XOR2>(a); b += dpp<XOR2>(b);
  a += dppz<0x142, 0xa>(a); b += dppz<0x142, 0xa>(b);
  a += dppz<0x143, 0xc>(a); b += dppz<0x143, 0xc>(b);
}
__device__ __forceinline__ void st4(unsigned v, rsrc_t r, unsigned vo, unsigned so) { __builtin_amdgcn_raw_buffer_store_b32(v, r, vo, so, AUX_SC1); }
__device__ __forceinline__ int sfresh(int x) { asm volatile("" : "+s"(x)); return x; }

struct Ctx {
  rsrc_t rws, rwo, rwq;
  LAS char* L;
  int wave, hk, s;
};

__device__ __forceinline__ f32x4 qk_mm(v4u k0, v4u k1, const v4u (&q)[4]) {
  return mfma(k1, q[1], mfma(k0, q[0], f32x4{0.f, 0.f, 0.f, 0.f}));
}
__device__ __forceinline__ float qk_red(f32x4 d) { return ((dpp<0x00>(d[0]) + dpp<0x55>(d[1])) + dpp<0xAA>(d[2])) + dpp<0xFF>(d[3]); }
__device__ __forceinline__ unsigned q_off(int lane) { return ((lane >> 3) & 1) * 512 + (lane >> 4) * 64 + (lane & 3) * 16; }

__device__ __forceinline__ unsigned wo_base(const Ctx& c) { return (c.hk * 512 + c.wave * 64) * 896 + (c.s < 14 ? c.s : 0) * 64; }
__device__ __forceinline__ unsigned wo_off(int lane) { return (((lane >> 2) & 1) * 32 + ((lane >> 5) & 1) * 4 + ((lane >> 4) & 1) * 2 + ((lane >> 3) & 1)) * 896 + (lane & 3) * 16; }
__device__ __forceinline__ void load_wo(const Ctx& c, int lane, v4u (&w)[4]) {
  const unsigned so = wo_base(c), vo = wo_off(lane);
#pragma unroll
  for (int i = 0; i < 4; ++i) w[i] = ld16<AUX_W>(c.rwo, vo, so + i * 8 * 896);
}
__device__ __forceinline__ void load_wq(const Ctx& c, int lane, v4u (&w)[4]) {
  const unsigned so = (c.wave < 7 ? c.wave : 0) * 64 * 8192 + c.hk * 1024 + c.s * 64;
  const unsigned vo = (lane >> 2) * 32768 + (lane & 3) * 16;
#pragma unroll
  for (int i = 0; i < 4; ++i) w[i] = ld16<AUX_W>(c.rwq, vo, so + i * 8192);
}
__device__ __forceinline__ void unpack_w(const v4u (&w)[4], float (&f)[32]) {
#pragma unroll
  for (int i = 0; i < 4; ++i)
#pragma unroll
    for (int e = 0; e < 4; ++e) { f[i * 8 + 2 * e] = bflo(w[i][e]); f[i * 8 + 2 * e + 1] = bfhi(w[i][e]); }
#pragma unroll
  for (int i = 0; i < 32; i += 8)
    asm volatile("" : "+v"(f[i]), "+v"(f[i + 1]), "+v"(f[i + 2]), "+v"(f[i + 3]), "+v"(f[i + 4]), "+v"(f[i + 5]), "+v"(f[i + 6]), "+v"(f[i + 7]));
}
__device__ __forceinline__ void gemv_tile(const float (&x)[4], const float (&f)[32], float& r0, float& r1) {
  float u[8];
#pragma unroll
  for (int e = 0; e < 8; ++e) u[e] = fmaf(x[3], f[24 + e], fmaf(x[2], f[16 + e], fmaf(x[1], f[8 + e], x[0] * f[e])));
  const float v0 = tr32(u[0], u[4]), v1 = tr32(u[1], u[5]), v2 = tr32(u[2], u[6]), v3 = tr32(u[3], u[7]);
  r0 = tr16(v0, v2); r1 = tr16(v1, v3);
  r0 += dpp<ROR8>(r0); r1 += dpp<ROR8>(r1);
  r0 += dpp<ROR4>(r0); r1 += dpp<ROR4>(r1);
}
__device__ __forceinline__ unsigned gemv_col(int lane) { return (lane & 3) * 8 + (lane >> 5) * 4 + ((lane >> 4) & 1) * 2; }
typedef float f32x2 __attribute__((ext_vector_type(2)));

__device__ __forceinline__ void softmax16(const float (&s)[16], unsigned (&pp)[16], float& m, float& l) {
  float mx = s[0];
#pragma unroll
  for (int j = 1; j < 16; ++j) mx = fmaxf(mx, s[j]);
  mx = maxx32(maxx16(mx));
  m = mx * SCL;
  float sum = 0.f;
#pragma unroll
  for (int j = 0; j < 16; ++j) { const float e = __builtin_amdgcn_exp2f(fmaf(s[j], SCL, -m)); sum += e; pp[j] = cvtpk(e, e); }
  l = sumx32(sumx16(sum));
}

__device__ __forceinline__ void finish_pass(const Ctx& c, int lane, f32x4 a0, f32x4 a1, float m, float l, unsigned slot) {
  lane = fresh(lane);
  const int wave = sfresh(c.wave);
  const unsigned G = lane >> 4, cc = lane & 15;
  const unsigned o = L_MRG + wave * 2048 + (G >> 1) * 1024 + cc * 32 + (G & 1) * 16;
  if ((lane & 0x37) == 0) {
    const unsigned h = lane >> 3;
    *lf(c.L, L_MST + h * 32 + wave * 4) = m; *lf(c.L, L_MST + 64 + h * 32 + wave * 4) = l;
  }
  *lf4(c.L, o) = a0; *lf4(c.L, o + 512) = a1;
  const unsigned h = wave >> 2;
  LAS char* const ms = c.L + L_MST + h * 32 + (lane & 7) * 4;
  LAS char* const mr = c.L + L_MRG + wave * 256 + lane * 4;
  wg_bar();
  const float mw = *(LAS float*)ms, lw = *(LAS float*)(ms + 64);
  float v[8];
#pragma unroll
  for (int w = 0; w < 8; ++w) v[w] = *(LAS float*)(mr + w * 2048);
  float M = fmaxf(mw, dpp<HMIR>(mw)); M = fmaxf(M, dpp<XOR1>(M)); M = fmaxf(M, dpp<XOR2>(M));
  const float sc = __builtin_amdgcn_exp2f(mw - M);
  const float Ls = sum8(sc * lw);
  float oa = 0.f, ob = 0.f;
#pragma unroll
  for (int w = 0; w < 8; w += 2) {
    oa = fmaf(asf(__builtin_amdgcn_readlane(asu(sc), w)), v[w], oa);
    ob = fmaf(asf(__builtin_amdgcn_readlane(asu(sc), w + 1)), v[w + 1], ob);
  }
  const float ov = (oa + ob) * __builtin_amdgcn_rcpf(Ls);
  const unsigned pk = nosent(cvtpk(ov, dpp<XOR1>(ov)));
  if ((lane & 1) == 0) st4(pk, c.rws, lane * 2, slot + wave * 128);
  if ((wave & 3) == 0 && lane == 0) st4(nosent(asu(M + __builtin_amdgcn_logf(Ls))), c.rws, 0, slot + 1024 + h * 128);
}

__device__ __forceinline__ unsigned part_slot(int bt, int pass, int hk, int s) { return WS_PART + ((((unsigned)bt * 2 + pass) * 8 + hk) * 16 + s) * PSL; }

template <int NR>
__device__ __forceinline__ void front(const Params& p, const Ctx& c, int lane, int bt0, int pass, unsigned lb, v4u (&w)[4]) {
  if (c.s >= 14) { load_wq(c, fresh(lane), w); return; }
  lane = fresh(lane);
  const int wave = sfresh(c.wave), hk = sfresh(c.hk), cs = sfresh(c.s);
  const int pr = 7 - wave;
  const int sb = 8 + (pass ? 4 : bt0 >> 1) * 24; (void)sb;
  {
    float wf[32];
    unpack_w(w, wf);
    v4u g0[NR], g1[NR]; unsigned e0[NR], e1[NR];
    const unsigned vo = (lane >> 3) * PSL + (lane & 7) * 16, ve = (lane >> 3) * PSL;
    LAS char* const r1 = c.L + lb + FR_RED1 + wave * 128 + gemv_col(lane) * 4;
    for (unsigned sp = 0;; ++sp) {
      CFENCE();
      unsigned mx = 0;
#pragma unroll
      for (int r = 0; r < NR; ++r) {
        const unsigned so = part_slot(bt0 + r, pass, hk, 0);
        e0[r] = ld4<AUX_SC1>(c.rws, ve, so + 1024 + (wave >> 2) * 128); e1[r] = ld4<AUX_SC1>(c.rws, ve, so + 1024 + (wave >> 2) * 128 + 8 * PSL);
        g0[r] = ld16<AUX_SC1>(c.rws, vo, so + wave * 128); g1[r] = ld16<AUX_SC1>(c.rws, vo, so + wave * 128 + 8 * PSL);
      }
#pragma unroll
      for (int r = 0; r < NR; ++r) mx = max(max(mx, max(e0[r], e1[r])), max(umax4(g0[r]), umax4(g1[r])));
      if (__builtin_amdgcn_ballot_w64(mx == SENT) == 0 || sp > SPIN_CAP) break;
      __builtin_amdgcn_s_sleep(SLP_A);
    }
    STAMP(sb + 2);
#pragma unroll
    for (int r = 0; r < NR; ++r) {
      float M = fmaxf(asf(e0[r]), asf(e1[r]));
      M = fmaxf(M, dpp<ROR8>(M)); M = maxx32(maxx16(M));
      const float x0 = __builtin_amdgcn_exp2f(asf(e0[r]) - M), x1 = __builtin_amdgcn_exp2f(asf(e1[r]) - M);
      float a[8];
#pragma unroll
      for (int e = 0; e < 4; ++e) {
        a[2 * e] = fmaf(x1, bflo(g1[r][e]), x0 * bflo(g0[r][e]));
        a[2 * e + 1] = fmaf(x1, bfhi(g1[r][e]), x0 * bfhi(g0[r][e]));
      }
      float S = x0 + x1; S += dpp<ROR8>(S);
      const float b0 = tr32(a[0], a[4]), b1 = tr32(a[1], a[5]), b2 = tr32(a[2], a[6]), b3 = tr32(a[3], a[7]);
      S = sumx32(S);
      float c0 = tr16(b0, b2), c1 = tr16(b1, b3);
      S = sumx16(S);
      c0 += dpp<ROR8>(c0); c1 += dpp<ROR8>(c1);
      const float o = ((lane & 8) ? c1 : c0) * __builtin_amdgcn_rcpf(S);
      const float xq[4] = {dpp<0x00>(o), dpp<0x55>(o), dpp<0xAA>(o), dpp<0xFF>(o)};
      float y0, y1;
      gemv_tile(xq, wf, y0, y1);
      if ((lane & 12) == 0) *(LAS f32x2*)(r1 + r * FR_ROW) = f32x2{y0, y1};
    }
  }
  const int t4 = (wave < 7 ? wave * 64 + lane : 0) * 8;
  const v2u pw1 = *(const v2u*)((const char*)p.w1 + t4), pb1 = *(const v2u*)((const char*)p.b1 + t4), pw2 = *(const v2u*)((const char*)p.w2 + t4);
  const float b2 = bflo(*(const unsigned short*)p.b2);
  STAMP(sb + 3);
  wg_bar();
  if (pr < NR) {
    LAS char* const rr = c.L + lb + pr * FR_ROW + FR_RED1 + (lane >> 5) * 512 + (lane & 31) * 4;
    const float a = sumx32((*(LAS float*)rr + *(LAS float*)(rr + 128)) + (*(LAS float*)(rr + 256) + *(LAS float*)(rr + 384)));
    if (lane < 32) st4(nosent(asu(a)), c.rws, lane * 4, WS_CB + ((bt0 + pr) * 2 + pass) * CBR + hk * 1792 + cs * 128);
  }
  STAMP(sb + 4);
  const unsigned cidx = hk * 14 + cs;
  float w1f[4] = {bflo(pw1[0]), bfhi(pw1[0]), bflo(pw1[1]), bfhi(pw1[1])};
  float b1f[4] = {bflo(pb1[0]), bfhi(pb1[0]), bflo(pb1[1]), bfhi(pb1[1])};
  float w2f[4] = {bflo(pw2[0]), bfhi(pw2[0]), bflo(pw2[1]), bfhi(pw2[1])};
  asm volatile("" : "+v"(w1f[0]), "+v"(w1f[1]), "+v"(w1f[2]), "+v"(w1f[3]), "+v"(b1f[0]), "+v"(b1f[1]), "+v"(b1f[2]), "+v"(b1f[3]));
  asm volatile("" : "+v"(w2f[0]), "+v"(w2f[1]), "+v"(w2f[2]), "+v"(w2f[3]));
  load_wq(c, lane, w);
  float sh[NR];
  if (wave < 7) {
    v4u g[NR][2]; unsigned rs[NR], r0[NR];
    const unsigned vo = (lane >> 4) * (2 * 1792) + (lane & 15) * 16;
    const unsigned dl = (lane & 15) * 16 + (lane >> 5) * 8 + ((lane >> 4) & 1) * 4;
    __builtin_amdgcn_s_sleep(SLP_B);
    for (unsigned sp = 0;; ++sp) {
      CFENCE();
      unsigned mx = 0;
#pragma unroll
      for (int r = 0; r < NR; ++r) {
        const unsigned so = WS_CB + ((bt0 + r) * 2 + pass) * CBR + wave * 256;
        const unsigned sr = pass ? WS_XD + ((bt0 + r) * XD_REP + (hk % XD_REP)) * 1792 : WS_X0 + (bt0 + r) * 1792;
        rs[r] = ld4<AUX_SC1>(c.rws, dl, sr + wave * 256); r0[r] = ld4<AUX_SC1>(c.rws, 0, sr);
        g[r][0] = ld16<AUX_SC1>(c.rws, vo, so); g[r][1] = ld16<AUX_SC1>(c.rws, vo + 1792, so);
      }
#pragma unroll
      for (int r = 0; r < NR; ++r) mx = max(max(mx, max(rs[r], r0[r])), max(umax4(g[r][0]), umax4(g[r][1])));
      if (__builtin_amdgcn_ballot_w64(mx == SENT) == 0 || sp > SPIN_CAP) break;
      __builtin_amdgcn_s_sleep(1);
    }
    STAMP(sb + 5);
#pragma unroll
    for (int r = 0; r < NR; ++r) {
      const float t0 = tr32(asf(g[r][0][0]) + asf(g[r][1][0]), asf(g[r][0][2]) + asf(g[r][1][2]));
      const float t1 = tr32(asf(g[r][0][1]) + asf(g[r][1][1]), asf(g[r][0][3]) + asf(g[r][1][3]));
      const float x1 = asf(rs[r]) + tr16(t0, t1);
      *lf(c.L, lb + r * FR_ROW + FR_X1 + wave * 256 + dl) = x1;
      sh[r] = asf(r0[r]);
      float s1 = x1 - sh[r], s2 = s1 * s1;
      wsum2(s1, s2);
      if (lane == 63) *(LAS f32x2*)(c.L + lb + r * FR_ROW + FR_ST + wave * 8) = f32x2{s1, s2};
    }
  }
  STAMP(sb + 6);
  wg_bar();
  if (wave < 7) {
#pragma unroll
    for (int r = 0; r < NR; ++r) {
      const unsigned rb = lb + r * FR_ROW;
      const f32x4 a0 = *lf4(c.L, rb + FR_ST), a1 = *lf4(c.L, rb + FR_ST + 16), a2 = *lf4(c.L, rb + FR_ST + 32);
      const f32x2 a3 = *(LAS f32x2*)(c.L + rb + FR_ST + 48);
      const f32x4 x1 = *lf4(c.L, rb + FR_X1 + cidx * 16);
      const float md = (((a0[0] + a0[2]) + (a1[0] + a1[2])) + ((a2[0] + a2[2]) + a3[0])) * (1.f / 448.f);
      const float var = fmaxf(fmaf(-md, md, (((a0[1] + a0[3]) + (a1[1] + a1[3])) + ((a2[1] + a2[3]) + a3[1])) * (1.f / 448.f)), 0.f);
      const float r1 = __builtin_amdgcn_rsqf(var + EPS);
      const float r2 = __builtin_amdgcn_rsqf(var * r1 * r1 + EPS);
      const float rr = r1 * r2;
      float y[4];
#pragma unroll
      for (int j = 0; j < 4; ++j) {
        const float hv = ((x1[j] - sh[r]) - md) * rr;
        float a = 0.f;
#pragma unroll
        for (int f = 0; f < 4; ++f) {
          const float u = fmaf(hv, w1f[f], b1f[f]);
          const float t = u * fmaf(u * u, -0.10294324f, -2.3022082f);
          a = fmaf(u * w2f[f], __builtin_amdgcn_rcpf(1.f + __builtin_amdgcn_exp2f(t)), a);
        }
        y[j] = a;
      }
      const float z = sum16(tr16(tr32(y[0], y[2]), tr32(y[1], y[3])));
      if ((lane & 15) == 0) *lf(c.L, rb + FR_RED2 + wave * 16 + (lane >> 4) * 4) = z;
      if (NR > 1) SCHED();
    }
  }
  STAMP(sb + 7);
  wg_bar();
  if (pr < NR && lane < 4 * XD_REP) {
    LAS char* const rb = c.L + lb + pr * FR_ROW + (lane & 3) * 4;
    float a[7];
#pragma unroll
    for (int ww = 0; ww < 7; ++ww) a[ww] = *(LAS float*)(rb + FR_RED2 + ww * 16);
    const float x2 = *(LAS float*)(rb + FR_X1 + cidx * 16) + ((((a[0] + a[1]) + (a[2] + a[3])) + ((a[4] + a[5]) + a[6])) + b2);
    if (pass == 0) st4(nosent(asu(x2)), c.rws, (lane >> 2) * 1792 + (lane & 3) * 4, WS_XD + (bt0 + pr) * XD_REP * 1792 + cidx * 16);
    else {
      const unsigned pk = cvtpk(x2, dpp<XOR1>(x2));
      if ((lane & 0x3d) == 0) *(unsigned*)((char*)p.out + (bt0 + pr) * 896 + cidx * 8 + lane * 2) = pk;
    }
  }
}

__device__ __forceinline__ void qproj1(const Ctx& c, int lane, int wave, const float (&d)[4], const float (&wf)[32], unsigned lb) {
  float s = tr32((d[0] + d[1]) + (d[2] + d[3]), fmaf(d[0], d[0], d[1] * d[1]) + fmaf(d[2], d[2], d[3] * d[3]));
  float y0, y1;
  gemv_tile(d, wf, y0, y1);
  s = sumx16(s); s += dpp<ROR8>(s); s += dpp<ROR4>(s);
  if ((lane & 12) == 0) *(LAS f32x2*)(c.L + lb + wave * 128 + gemv_col(lane) * 4) = f32x2{y0, y1};
  if ((lane & 31) == 0) *lf(c.L, lb + 1024 + (lane >> 5) * 32 + wave * 4) = s;
}
__device__ __forceinline__ void qproj2(const Ctx& c, int lane, unsigned lb, unsigned dst, float cs) {
  LAS char* const rr = c.L + lb + (lane >> 5) * 512 + (lane & 31) * 4;
  const float u0 = *(LAS float*)rr, u1 = *(LAS float*)(rr + 128), u2 = *(LAS float*)(rr + 256), u3 = *(LAS float*)(rr + 384);
  const f32x4 a0 = *lf4(c.L, lb + 1024), a1 = *lf4(c.L, lb + 1040), q0 = *lf4(c.L, lb + 1056), q1 = *lf4(c.L, lb + 1072);
  const float U = sumx32((u0 + u1) + (u2 + (lane < 32 ? u3 : 0.f)));
  const float md = (((a0[0] + a0[1]) + (a0[2] + a0[3])) + ((a1[0] + a1[1]) + a1[2])) * (1.f / 448.f);
  const float var = fmaxf(fmaf(-md, md, (((q0[0] + q0[1]) + (q0[2] + q0[3])) + ((q1[0] + q1[1]) + q1[2])) * (1.f / 448.f)), 0.f);
  const float q = fmaf(-md, cs, U) * __builtin_amdgcn_rsqf(var + EPS);
  const unsigned pk = nosent(cvtpk(q, dpp<XOR1>(q)));
  if ((lane & 0x21) == 0) st4(pk, c.rws, lane * 2, dst + c.s * 64);
}
__device__ __forceinline__ void colsum1(const Ctx& c, int lane, int wave, const float (&wf)[32]) {
  const float one[4] = {1.f, 1.f, 1.f, 1.f};
  float y0, y1;
  gemv_tile(one, wf, y0, y1);
  if (wave < 7 && (lane & 12) == 0) *(LAS f32x2*)(c.L + L_BK + wave * 128 + gemv_col(lane) * 4) = f32x2{y0, y1};
}
__device__ __forceinline__ float colsum2(const Ctx& c, int lane) {
  float a = 0.f;
#pragma unroll
  for (int ww = 0; ww < 7; ++ww) a += *lf(c.L, L_BK + ww * 128 + (lane & 31) * 4);
  return a;
}
__device__ __forceinline__ bool flags_ok(v4u f, unsigned long long tag) {
  return __builtin_amdgcn_ballot_w64(!(f[0] == (unsigned)tag && f[1] == (unsigned)(tag >> 32) && f[2] == (unsigned)tag && f[3] == (unsigned)(tag >> 32))) == 0;
}
__device__ __forceinline__ void wait_flags(const Ctx& c, int lane, unsigned so, unsigned long long tag) {
  for (unsigned sp = 0;; ++sp) {
    CFENCE();
    if (flags_ok(ld16<AUX_SC1>(c.rws, lane * 16, so), tag) || sp > SPIN_CAP) break;
    __builtin_amdgcn_s_sleep(24);
  }
  CFENCE();
}

__device__ __forceinline__ void fetch_q1(const Ctx& c, int lane, unsigned so, unsigned long long tag) {
  wait_flags(c, lane, WS_FLAGS, tag);
  *(LAS v4u*)(c.L + L_Q + lane * 16) = ld16<AUX_SC1>(c.rws, lane * 16, so);
  asm volatile("s_waitcnt lgkmcnt(0)" ::: "memory");
  *(volatile LAS unsigned long long*)(c.L + L_QF) = tag;
}

__global__ void __launch_bounds__(512, 2) fused_decode_kernel(Params p) {
  extern __shared__ __attribute__((aligned(16))) char smem[];
  Ctx c;
  c.L = (LAS char*)smem;
  const int tid = threadIdx.x;
  int lane = tid & 63;
  c.wave = __builtin_amdgcn_readfirstlane(tid >> 6);
  const int blk = blockIdx.x;
  const int grp = (blk >> 5) & 1, cc = (blk & 31) | ((blk >> 6) << 5);
  c.hk = cc & 7; c.s = cc >> 3;
  c.rws = mkrs(p.ws, WS_BYTES); c.rwo = mkrs(p.wo, 4096 * 448 * 2); c.rwq = mkrs(p.wq, 4096 * 448 * 2);
  const rsrc_t rk = mkrs(p.k, 1u << 28), rv = mkrs(p.v, 1u << 28);
  const unsigned long long t_start = __builtin_amdgcn_s_memrealtime();
  STAMP_INIT();
  STAMP(0);

  const v2u ep = ld8<AUX_SC1>(c.rws, 0, WS_EPOCH);
  const unsigned kvw = (c.hk * 8192 + c.s * 512 + c.wave * 64) * 512;
#define KV_SETUP() const int lk_ = fresh(lane); const unsigned ko = ((lk_ >> 2) & 3) * 512 + (lk_ >> 4) * 64 + (lk_ & 3) * 16, vo = (lk_ >> 4) * 512 + (lk_ & 15) * 16; \
    LAS char* const kl = c.L + L_K + c.wave * 16384 + lk_ * 16; (void)kl; (void)ko; (void)vo
#define K_LD(base, j) ld16<AUX_NT>(rk, ko + ((j) & 1) * 256 + (((j) >> 1) & 1) * 2048, (base) + ((j) >> 2) * 4096)
#define V_LD(base, j) ld16<AUX_NT>(rv, vo + ((j) & 1) * 256 + (((j) >> 1) & 1) * 2048, (base) + ((j) >> 2) * 4096)

  v4u K[16], V[32], aux[4];
  unsigned long long tag;
  int bt = grp;
  {
    if (grp == 1) {
      unsigned long long pc;
      asm volatile("s_getpc_b64 %0" : "=s"(pc));
      const unsigned pf = *(const __attribute__((address_space(1))) unsigned*)((pc & ~255ull) + (c.s * 8 + c.wave) * 256 + lane * 4);
      load_wq(c, lane, aux);
      const int xo = ((c.wave < 7 ? c.wave : 0) * 64 + (lane >> 2) * 4) * 2;
      v2u xr[8]; unsigned x0[8];
#pragma unroll
      for (int b = 0; b < 8; ++b) { xr[b] = *(const v2u*)((const char*)p.x + b * 896 + xo); x0[b] = *(const unsigned short*)((const char*)p.x + b * 896); }
      const unsigned xc = ((const unsigned short*)p.x)[(cc & 7) * 448 + (tid < 448 ? tid : 0)];
      STAMP(3);
      float wf[32];
      unpack_w(aux, wf);
      asm volatile("" :: "v"(pf));
      STAMP(4);
      colsum1(c, lane, c.wave, wf);
      const unsigned long long tg = mktag(ep);
      float cs = 0.f;
#pragma unroll
      for (int ph = 0; ph < 2; ++ph) {
        if (ph == 1) {
          const v4u sent = {SENT, SENT, SENT, SENT};
          for (unsigned o = WS_S0 + (cc * 512 + tid) * 16; o < WS_SEND; o += 128 * 512 * 16) st16(sent, c.rws, o, 0);
          if (cc < 8 && tid < 448) st4(nosent(xc << 16), c.rws, tid * 4, WS_X0 + cc * 1792);
        }
        if (c.wave < 7) {
#pragma unroll
          for (int b = 2 * ph; b < (ph ? 8 : 2); ++b) {
            const float sh = bflo(x0[b]);
            const float d[4] = {bflo(xr[b][0]) - sh, bfhi(xr[b][0]) - sh, bflo(xr[b][1]) - sh, bfhi(xr[b][1]) - sh};
            qproj1(c, lane, c.wave, d, wf, L_K + b * BK_ROW);
          }
        }
        wg_bar();
        if (ph == 0) { cs = colsum2(c, lane); if (tid < 32) *lf(c.L, L_CS + tid * 4) = cs; }
        if ((c.wave >= 2) == (ph == 1)) qproj2(c, lane, L_K + c.wave * BK_ROW, WS_Q1 + (c.wave * 8 + c.hk) * 1024, cs);
        asm volatile("s_waitcnt vmcnt(0)" ::: "memory");
        wg_bar();
        if (tid == 0) __builtin_amdgcn_raw_buffer_store_b64(v2u{(unsigned)tg, (unsigned)(tg >> 32)}, c.rws, cc * 8, WS_FLAGS + ph * 1024, AUX_SC1);
        STAMP(ph ? 5 : 1);
      }
      if (c.wave == 0) fetch_q1(c, lane, WS_Q1 + (8 + c.hk) * 1024, tg);
      while (__builtin_amdgcn_s_memrealtime() - t_start < STAGGER_TICKS) __builtin_amdgcn_s_sleep(4);
    }
    const unsigned base = bt * (32u << 20) + kvw;
    KV_SETUP();
    SCHED();
#pragma unroll
    for (int j = 0; j < 16; ++j) K[j] = K_LD(base, j);
#pragma unroll
    for (int j = 0; j < 16; ++j) V[j] = K_LD(base, 16 + j);
#pragma unroll
    for (int j = 16; j < 32; ++j) V[j] = V_LD(base, j);
    SCHED();
    STAMP(6);
    tag = mktag(ep);
    if (grp == 0 && c.wave == 0) {
      while (__builtin_amdgcn_s_memrealtime() - t_start < FIRST_POLL_TICKS) __builtin_amdgcn_s_sleep(4);
      fetch_q1(c, lane, WS_Q1 + c.hk * 1024, tag);
    }
    for (unsigned sp = 0; sp < 64 * SPIN_CAP; ++sp) {
      CFENCE();
      if (*(volatile LAS unsigned long long*)(c.L + L_QF) == tag) break;
      __builtin_amdgcn_s_sleep(2);
    }
    CFENCE();
    STAMP(7);
    aux[0] = *(LAS v4u*)(c.L + L_Q + q_off(lane));
    aux[1] = *(LAS v4u*)(c.L + L_Q + q_off(lane) + 256);
    asm volatile("" : "+v"(aux[0]), "+v"(aux[1]));
    aux[2] = aux[0]; aux[3] = aux[1];
  }

#pragma unroll 1
  for (int it = 0;; ++it) {
    const bool fin = it == 4;
    if (!fin) {
      const unsigned base = bt * (32u << 20) + kvw;
      KV_SETUP();
      float s[16];
#pragma unroll
      for (int j = 0; j < 8; ++j) {
        const f32x4 d0 = qk_mm(V[2 * j], V[2 * j + 1], aux), d1 = qk_mm(K[2 * j], K[2 * j + 1], aux);
        *(LAS v4u*)(kl + (2 * j) * 1024) = V[2 * j]; *(LAS v4u*)(kl + (2 * j + 1) * 1024) = V[2 * j + 1];
        s[8 + j] = qk_red(d0); s[j] = qk_red(d1);
        PIN2(s[j], s[8 + j]);
        SCHED();
        V[2 * j] = V_LD(base, 2 * j); V[2 * j + 1] = V_LD(base, 2 * j + 1);
        SCHED();
      }
      load_wo(c, fresh(lane), aux);
      SCHED();
      unsigned pp[16]; float m, l;
      softmax16(s, pp, m, l);
      const int ln = fresh(lane);
      const unsigned hm = (ln & 1) ? 0xffff0000u : 0x0000ffffu, wi = (ln >> 1) & 3;
      const v4u msk = {wi == 0 ? hm : 0u, wi == 1 ? hm : 0u, wi == 2 ? hm : 0u, wi == 3 ? hm : 0u};
      f32x4 a0 = {0.f, 0.f, 0.f, 0.f}, a1 = {0.f, 0.f, 0.f, 0.f};
#pragma unroll
      for (int jj = 0; jj < 16; ++jj) {
        const int j = (jj + 8) & 15;
        const v4u A = msk & pp[j];
        a0 = mfma(A, V[2 * j], a0); a1 = mfma(A, V[2 * j + 1], a1);
      }
      if (it == 0) wait_flags(c, fresh(lane), WS_FLAGS + 1024, tag);
      STAMP(8 + it * 24 + 0);
      finish_pass(c, lane, a0, a1, m, l, part_slot(bt, 0, c.hk, c.s));
      STAMP(8 + it * 24 + 1);
    }
    front<1>(p, c, lane, bt, fin ? 1 : 0, L_FR, aux);
    asm volatile("s_cmp_eq_u32 %0, 4\n\ts_cbranch_scc0 .Lgo_on%=\n\ts_endpgm\n.Lgo_on%=:" :: "s"(it) : "scc", "memory");
    STAMP(8 + it * 24 + 8);
    {
      const int ln = fresh(lane), wave = sfresh(c.wave);
      float wf[32];
      unpack_w(aux, wf);
      if (it == 0 && grp == 0) {
        colsum1(c, ln, wave, wf);
        wg_bar();
        if (wave == 7) *lf(c.L, L_CS + (ln & 31) * 4) = colsum2(c, ln);
        wg_bar();
      }
      if (wave < 7) {
        __builtin_amdgcn_s_sleep(SLP_D);
        v4u xv; unsigned x0;
        const unsigned so = WS_XD + (bt * XD_REP + (c.hk % XD_REP)) * 1792;
        for (unsigned sp = 0;; ++sp) {
          CFENCE();
          x0 = ld4<AUX_SC1>(c.rws, 0, so);
          xv = ld16<AUX_SC1>(c.rws, (ln >> 2) * 16, so + wave * 256);
          if (__builtin_amdgcn_ballot_w64(max(umax4(xv), x0) == SENT) == 0 || sp > SPIN_CAP) break;
          __builtin_amdgcn_s_sleep(1);
        }
        STAMP(8 + it * 24 + 9);
        const float d[4] = {asf(xv[0]) - asf(x0), asf(xv[1]) - asf(x0), asf(xv[2]) - asf(x0), asf(xv[3]) - asf(x0)};
        qproj1(c, ln, wave, d, wf, L_BK);
      }
      STAMP(8 + it * 24 + 10);
      wg_bar();
      const unsigned sq = WS_Q2 + (bt * 8 + c.hk) * 1024;
      if (wave == 7) qproj2(c, ln, L_BK, sq, *lf(c.L, L_CS + (ln & 31) * 4));
      else __builtin_amdgcn_s_sleep(3);
      STAMP(8 + it * 24 + 11);
      __builtin_amdgcn_s_sleep(SLP_C);
      const unsigned qo = q_off(ln);
      for (unsigned sp = 0;; ++sp) {
        CFENCE();
        aux[0] = ld16<AUX_SC1>(c.rws, qo, sq); aux[1] = ld16<AUX_SC1>(c.rws, qo + 256, sq);
        if (__builtin_amdgcn_ballot_w64(max(umax4(aux[0]), umax4(aux[1])) == SENT) == 0 || sp > SPIN_CAP) break;
        __builtin_amdgcn_s_sleep(1);
      }
      STAMP(8 + it * 24 + 12);
    }
    const bool nxt = it < 3;
    {
      const unsigned base = (nxt ? bt + 2 : bt) * (32u << 20) + kvw;
      const int gate = __builtin_amdgcn_readfirstlane(nxt ? 1 : 0);
      float s[16];
      {
        KV_SETUP();
        v4u kb[4];
#pragma unroll
        for (int u = 0; u < 4; ++u) kb[u] = *(LAS v4u*)(kl + u * 1024);
#pragma unroll
        for (int j = 0; j < 8; ++j) s[j] = qk_red(qk_mm(K[2 * j], K[2 * j + 1], aux));
        asm volatile("" : "+v"(s[0]), "+v"(s[1]), "+v"(s[2]), "+v"(s[3]), "+v"(s[4]), "+v"(s[5]), "+v"(s[6]), "+v"(s[7]));
        SKIP_BEGIN(gate, 0, rk, ko);
#pragma unroll
        for (int j = 0; j < 16; ++j) K[j] = K_LD(base, j);
        SKIP_END(0);
#pragma unroll
        for (int j = 0; j < 8; j += 2) {
          const f32x4 d0 = qk_mm(kb[0], kb[1], aux), d1 = qk_mm(kb[2], kb[3], aux);
          SCHED();
          if (j < 6) {
#pragma unroll
            for (int u = 0; u < 4; ++u) kb[u] = *(LAS v4u*)(kl + (2 * j + 4 + u) * 1024);
          }
          s[8 + j] = qk_red(d0); s[9 + j] = qk_red(d1);
          PIN2(s[8 + j], s[9 + j]);
        }
      }
      STAMP(8 + it * 24 + 13);
      SCHED();
      {
        const int lq = fresh(lane);
        const unsigned va = nxt ? q_off(lq) : wo_off(lq), st = nxt ? 256 : 8 * 896;
        const rsrc_t ra = mkrs(nxt ? p.ws : p.wo, nxt ? WS_BYTES : 4096 * 448 * 2);
        const unsigned so = nxt ? WS_Q1 + ((bt + 2) * 8 + c.hk) * 1024 : wo_base(c);
        aux[0] = ld16<AUX_SC1>(ra, va, so);
        aux[1] = ld16<AUX_SC1>(ra, va, so + st);
        aux[2] = ld16<AUX_SC1>(ra, va, so + (nxt ? 0 : 2 * st));
        aux[3] = ld16<AUX_SC1>(ra, va, so + (nxt ? 0 : 3 * st));
      }
      SCHED();
      unsigned pp[16]; float m, l;
      softmax16(s, pp, m, l);
      const int ln = fresh(lane);
      const unsigned hm = (ln & 1) ? 0xffff0000u : 0x0000ffffu, wi = (ln >> 1) & 3;
      const v4u msk = {wi == 0 ? hm : 0u, wi == 1 ? hm : 0u, wi == 2 ? hm : 0u, wi == 3 ? hm : 0u};
      f32x4 a0 = {0.f, 0.f, 0.f, 0.f}, a1 = {0.f, 0.f, 0.f, 0.f};
      KV_SETUP();
#pragma unroll
      for (int j = 0; j < 8; ++j) { const v4u A = msk & pp[j]; a0 = mfma(A, V[2 * j], a0); a1 = mfma(A, V[2 * j + 1], a1); }
      PIN2(a0, a1);
      SKIP_BEGIN(gate, 1, rk, ko);
#pragma unroll
      for (int j = 0; j < 16; ++j) V[j] = K_LD(base, 16 + j);
      SKIP_END(1);
#pragma unroll
      for (int j = 8; j < 16; ++j) { const v4u A = msk & pp[j]; a0 = mfma(A, V[2 * j], a0); a1 = mfma(A, V[2 * j + 1], a1); }
      PIN2(a0, a1);
      SKIP_BEGIN(gate, 2, rv, vo);
#pragma unroll
      for (int j = 16; j < 32; ++j) V[j] = V_LD(base, j);
      SKIP_END(2);
      asm volatile("s_cmp_eq_u32 %0, 0\n\ts_cbranch_scc0 .Lnodrain%=\n\ts_waitcnt vmcnt(0)\n.Lnodrain%=:" :: "s"(gate) : "scc", "memory");
      STAMP(8 + it * 24 + 15);
      finish_pass(c, lane, a0, a1, m, l, part_slot(bt, 1, c.hk, c.s));
      STAMP(8 + it * 24 + 16);
    }
    if (nxt) { bt += 2; wg_bar(); }
    else if (grp == 0) break;
  }
  front<7>(p, c, lane, 0, 1, L_K, aux);
  if (cc == 0 && tid == 0) __builtin_amdgcn_raw_buffer_store_b64(v2u{(unsigned)tag, (unsigned)(tag >> 32)}, c.rws, 0, WS_EPOCH, AUX_SC1);
  STAMP_DUMP();
}

}

extern "C" void kernel_launch(void* const* d_in, const int* in_sizes, int n_in, void* d_out, int out_size, void* d_ws, size_t ws_size,
                              hipStream_t stream) {
  (void)in_sizes; (void)n_in; (void)out_size; (void)ws_size;
  Params p;
  p.x = d_in[0]; p.k = d_in[1]; p.v = d_in[2]; p.wq = d_in[3]; p.wo = d_in[4];
  p.w1 = d_in[6]; p.b1 = d_in[7]; p.w2 = d_in[8]; p.b2 = d_in[9];
  p.out = d_out; p.ws = d_ws;
  (void)hipFuncSetAttribute((const void*)fused_decode_kernel, hipFuncAttributeMaxDynamicSharedMemorySize, LDS_BYTES);
  hipLaunchKernelGGL(fused_decode_kernel, dim3(256), dim3(512), LDS_BYTES, stream, p);
}
```

```cpp
#include <hip/hip_runtime.h>
#include <hip/hip_bf16.h>
#include <stdint.h>

#define LAS __attribute__((address_space(3)))
typedef unsigned v4u __attribute__((ext_vector_type(4)));
typedef unsigned v2u __attribute__((ext_vector_type(2)));
typedef float f32x4 __attribute__((ext_vector_type(4)));
typedef float f32x2 __attribute__((ext_vector_type(2)));
typedef __bf16 bf16x2_t __attribute__((ext_vector_type(2)));
typedef __bf16 bf16x8_t __attribute__((ext_vector_type(8)));
typedef __amdgpu_buffer_rsrc_t rsrc_t;
typedef unsigned long long u64;

namespace {

constexpr unsigned SENT = 0xFFFFFFFFu;
constexpr int AUX_NT = 2, AUX_SC1 = 16;
constexpr unsigned SPIN_CAP = 1u << 16;
constexpr float EPS = 1e-5f;
constexpr float SCL = 0.0625f * 1.4426950408889634f;

#define AUX_W 0
#define AUX_WO 0
#define XD_REP 8
#define SLP_A0 3
#define SLP_A 8
#define SLP_B 10
#define SLP_D 8
#define SLP_C 6
#define XS {0, 0, 0, 0}
#define XH {0, 0, 0}
#define XW {0, 0, 0}
#define LD_WIN 4
#define P1_MODE 0
#define P1_NB 12
#define P2_N1 2
#define P2_N2 4
#define TOK_CU 0
#define TOK_J 6
#define SLP_R 4
#define SLP_RB 8
#define SLP_RD 8
#define FIRST_POLL_TICKS 550

constexpr unsigned WS_EPOCH = 0;
constexpr unsigned WS_FLAGS = 1024;
constexpr unsigned WS_X0 = 4096;
constexpr unsigned WS_Q1 = 32768;
constexpr unsigned WS_TOK = 131072;
constexpr unsigned WS_S0 = 262144;
constexpr unsigned PSL = 1152;
constexpr unsigned WS_PART = WS_S0;
constexpr unsigned PART_BYTES = 8u * 2 * 8 * 16 * PSL;
constexpr unsigned WS_CB = WS_PART + PART_BYTES;
constexpr unsigned CBR = 8 * 1792;
constexpr unsigned WS_XD = WS_CB + 16 * CBR;
constexpr unsigned XD_BYTES = 8u * XD_REP * 1792;
constexpr unsigned WS_Q2 = WS_XD + XD_BYTES;
constexpr unsigned WS_SEND = WS_Q2 + 65536;
constexpr unsigned WS_STAMP = 8u << 20;
constexpr unsigned WS_BYTES = 16u << 20;
static_assert(WS_SEND <= WS_STAMP, "");
static_assert((WS_SEND - WS_S0) % 16 == 0, "");

constexpr unsigned L_MRG = 0;
constexpr unsigned L_MST = 16384;
constexpr unsigned L_CS = L_MST + 128;
constexpr unsigned L_FR = L_CS + 128;
constexpr unsigned FR_RED1 = 0, FR_X1 = 1024, FR_ST = 1024 + 1792, FR_RED2 = FR_ST + 64, FR_ROW = FR_RED2 + 128;
constexpr unsigned L_BK = L_FR + FR_ROW;
constexpr unsigned BK_ROW = 1024 + 64;
constexpr unsigned L_Q = L_BK + BK_ROW, L_QF = L_Q + 1024;
constexpr unsigned L_END = L_QF + 16;
constexpr unsigned L_K = 32768;
constexpr unsigned LDS_BYTES = 163840;
static_assert(L_END <= L_K, "");
static_assert(FR_ROW % 16 == 0 && L_FR % 16 == 0 && L_BK % 16 == 0, "");

struct Params {
  const void* x; const void* k; const void* v; const void* wq; const void* wo;
  const void* w1; const void* b1; const void* w2; const void* b2;
  void* out; void* ws;
};

#define SCHED() __builtin_amdgcn_sched_barrier(0)
#define PIN1(a) asm volatile("" : "+v"(a))
#define PIN2(a, b) asm volatile("" : "+v"(a), "+v"(b))
#define SPIN1(a) asm volatile("" : "+s"(a))
#define CFENCE() asm volatile("" ::: "memory")

__device__ __forceinline__ float asf(unsigned u) { return __builtin_bit_cast(float, u); }
__device__ __forceinline__ unsigned asu(float f) { return __builtin_bit_cast(unsigned, f); }
__device__ __forceinline__ float bflo(unsigned u) { return asf(u << 16); }
__device__ __forceinline__ float bfhi(unsigned u) { return asf(u & 0xffff0000u); }
__device__ __forceinline__ f32x2 bf2(unsigned u) { return f32x2{bflo(u), bfhi(u)}; }
__device__ __forceinline__ f32x2 fma2(f32x2 a, f32x2 b, f32x2 c) { return __builtin_elementwise_fma(a, b, c); }
__device__ __forceinline__ f32x2 sp2(float a) { return f32x2{a, a}; }
#pragma float_control(precise, off, push)
__device__ __forceinline__ float fmx(float a, float b) { return __builtin_fmaxf(a, b); }
#pragma float_control(pop)
__device__ __forceinline__ unsigned cvtpk(float lo, float hi) {
  bf16x2_t r; r[0] = (__bf16)lo; r[1] = (__bf16)hi; return __builtin_bit_cast(unsigned, r);
}
template <int CTRL> __device__ __forceinline__ float dpp(float x) {
  return __builtin_bit_cast(float, __builtin_amdgcn_mov_dpp(__builtin_bit_cast(int, x), CTRL, 0xf, 0xf, true));
}
template <int CTRL, int RM> __device__ __forceinline__ float dppz(float x) {
  return __builtin_bit_cast(float, __builtin_amdgcn_update_dpp(0, __builtin_bit_cast(int, x), CTRL, RM, 0xf, true));
}
constexpr int XOR1 = 0xB1, XOR2 = 0x4E, HMIR = 0x141, ROR8 = 0x128, ROR4 = 0x124;
__device__ __forceinline__ float sum8(float x) { x += dpp<HMIR>(x); x += dpp<XOR1>(x); x += dpp<XOR2>(x); return x; }
__device__ __forceinline__ float sum16(float x) { x += dpp<ROR8>(x); return sum8(x); }
__device__ __forceinline__ float tr32(float a, float b) { auto s = __builtin_amdgcn_permlane32_swap(asu(a), asu(b), false, false); return asf(s[0]) + asf(s[1]); }
__device__ __forceinline__ float tr16(float a, float b) { auto s = __builtin_amdgcn_permlane16_swap(asu(a), asu(b), false, false); return asf(s[0]) + asf(s[1]); }
__device__ __forceinline__ float sumx16(float x) { return tr16(x, x); }
__device__ __forceinline__ float sumx32(float x) { return tr32(x, x); }
__device__ __forceinline__ float maxx16(float x) { auto s = __builtin_amdgcn_permlane16_swap(asu(x), asu(x), false, false); return fmx(asf(s[0]), asf(s[1])); }
__device__ __forceinline__ float maxx32(float x) { auto s = __builtin_amdgcn_permlane32_swap(asu(x), asu(x), false, false); return fmx(asf(s[0]), asf(s[1])); }
__device__ __forceinline__ f32x2 tr32p(f32x2 a, f32x2 b) {
  auto s = __builtin_amdgcn_permlane32_swap(asu(a[0]), asu(b[0]), false, false);
  auto t = __builtin_amdgcn_permlane32_swap(asu(a[1]), asu(b[1]), false, false);
  return f32x2{asf(s[0]), asf(t[0])} + f32x2{asf(s[1]), asf(t[1])};
}
__device__ __forceinline__ f32x2 tr16p(f32x2 a, f32x2 b) {
  auto s = __builtin_amdgcn_permlane16_swap(asu(a[0]), asu(b[0]), false, false);
  auto t = __builtin_amdgcn_permlane16_swap(asu(a[1]), asu(b[1]), false, false);
  return f32x2{asf(s[0]), asf(t[0])} + f32x2{asf(s[1]), asf(t[1])};
}
__device__ __forceinline__ unsigned pack_pair(float v) { return cvtpk(dpp<0xA0>(v), dpp<0xF5>(v)); }
__device__ __forceinline__ f32x4 mfma(v4u a, v4u b, f32x4 c) {
  return __builtin_amdgcn_mfma_f32_16x16x32_bf16(__builtin_bit_cast(bf16x8_t, a), __builtin_bit_cast(bf16x8_t, b), c, 0, 0, 0);
}
__device__ __forceinline__ unsigned umax4(v4u v) { return max(max(v[0], v[1]), max(v[2], v[3])); }
__device__ __forceinline__ bool pending(unsigned mx) { return __builtin_amdgcn_ballot_w64(mx == SENT) != 0; }
__device__ __forceinline__ unsigned nosent(unsigned u) { return min(u, 0xFFFFFFFEu); }
__device__ __forceinline__ void wg_bar() {
  asm volatile("s_waitcnt lgkmcnt(0)" ::: "memory");
  __builtin_amdgcn_s_barrier();
  CFENCE();
}
__device__ __forceinline__ int fresh(int) {
  int l;
  asm volatile("v_mbcnt_lo_u32_b32 %0, -1, 0\n\tv_mbcnt_hi_u32_b32 %0, -1, %0" : "=v"(l));
  __builtin_assume(l >= 0 && l < 64);
  return l;
}
__device__ __forceinline__ int sfresh(int x) { asm volatile("" : "+s"(x)); return x; }
__device__ __forceinline__ rsrc_t mkrs(const void* p, unsigned bytes) { return __builtin_amdgcn_make_buffer_rsrc((void*)p, 0, bytes, 0x00020000); }
template <int AUX> __device__ __forceinline__ v4u ld16(rsrc_t r, unsigned vo, unsigned so) { return __builtin_amdgcn_raw_buffer_load_b128(r, vo, so, AUX); }
template <int AUX> __device__ __forceinline__ v2u ld8(rsrc_t r, unsigned vo, unsigned so) { return __builtin_amdgcn_raw_buffer_load_b64(r, vo, so, AUX); }
template <int AUX> __device__ __forceinline__ unsigned ld4(rsrc_t r, unsigned vo, unsigned so) { return __builtin_amdgcn_raw_buffer_load_b32(r, vo, so, AUX); }
__device__ __forceinline__ void st16(v4u v, rsrc_t r, unsigned vo, unsigned so) { __builtin_amdgcn_raw_buffer_store_b128(v, r, vo, so, AUX_SC1); }
__device__ __forceinline__ void st8(u64 v, rsrc_t r, unsigned vo, unsigned so) { __builtin_amdgcn_raw_buffer_store_b64(v2u{(unsigned)v, (unsigned)(v >> 32)}, r, vo, so, AUX_SC1); }
__device__ __forceinline__ void st4(unsigned v, rsrc_t r, unsigned vo, unsigned so) { __builtin_amdgcn_raw_buffer_store_b32(v, r, vo, so, AUX_SC1); }
template <class T> __device__ __forceinline__ LAS T& lds(unsigned a) { return *(LAS T*)(size_t)a; }

#define STAMP(i) do {} while (0)
#define STAMP_INIT() do {} while (0)
#define STAMP_DUMP() do {} while (0)
#define STAMPF(i) do {} while (0)

__device__ __forceinline__ u64 mktag(v2u ep) {
  const u64 e = ((u64)(unsigned)__builtin_amdgcn_readfirstlane(ep[1]) << 32) | (unsigned)__builtin_amdgcn_readfirstlane(ep[0]);
  return e * 0x9E3779B97F4A7C15ull + 0x632BE59BD9B4E019ull;
}
#define SKIP_BEGIN(gate, id, rs, vo) do { SCHED(); asm volatile("s_cmp_eq_u32 %0, 0\n\ts_cbranch_scc0 .Lfd_run%c1\n\ts_nop 7\n\ts_branch .Lfd_skip%c1\n.Lfd_run%c1:" :: "s"(gate), "i"(id), "s"(rs), "v"(vo) : "scc"); SCHED(); } while (0)
#define SKIP_END(id) do { SCHED(); asm volatile(".Lfd_skip%c0:" :: "i"(id) : "scc"); SCHED(); } while (0)

struct Ctx {
  rsrc_t rws, rwo, rwq;
  int wave, hk, s;
};

__device__ __forceinline__ f32x4 qk_mm(v4u k0, v4u k1, const v4u (&q)[2]) { return mfma(k1, q[1], mfma(k0, q[0], f32x4{0.f, 0.f, 0.f, 0.f})); }
__device__ __forceinline__ float qk_red(f32x4 d) { return ((dpp<0x00>(d[0]) + dpp<0x55>(d[1])) + dpp<0xAA>(d[2])) + dpp<0xFF>(d[3]); }
__device__ __forceinline__ unsigned q_off(int lane) { return ((lane >> 3) & 1) * 512 + (lane >> 4) * 64 + (lane & 3) * 16; }

__device__ __forceinline__ unsigned wo_base(const Ctx& c) { return (c.hk * 512 + c.wave * 64) * 896 + (c.s < 14 ? c.s : 0) * 64; }
__device__ __forceinline__ unsigned wo_off(int lane) { return (((lane >> 2) & 1) * 32 + (lane >> 3)) * 896 + (lane & 3) * 16; }
__device__ __forceinline__ void load_wo(const Ctx& c, int lane, v4u (&w)[4]) {
  const unsigned so = wo_base(c), vo = wo_off(lane);
#pragma unroll
  for (int i = 0; i < 4; ++i) w[i] = ld16<AUX_WO>(c.rwo, vo, so + i * 8 * 896);
}
__device__ __forceinline__ unsigned wq_off(int lane) { return (lane >> 2) * 32768 + (lane & 3) * 16; }
__device__ __forceinline__ void load_wq2(const Ctx& c, unsigned vo, v4u (&w)[4], int i) {
  const unsigned so = (c.wave < 7 ? c.wave : 0) * 64 * 8192 + c.hk * 1024 + c.s * 64;
  w[i] = ld16<AUX_W>(c.rwq, vo, so + i * 8192); w[i + 1] = ld16<AUX_W>(c.rwq, vo, so + (i + 1) * 8192);
}
__device__ __forceinline__ void load_wq(const Ctx& c, int lane, v4u (&w)[4]) { load_wq2(c, wq_off(lane), w, 0); load_wq2(c, wq_off(lane), w, 2); }
__device__ __forceinline__ float dot2(unsigned a, unsigned b, float c) {
  return __builtin_amdgcn_fdot2_f32_bf16(__builtin_bit_cast(bf16x2_t, a), __builtin_bit_cast(bf16x2_t, b), c, false);
}
__device__ __forceinline__ void pair_rows(v4u (&w)[4]) {
#pragma unroll
  for (int h = 0; h < 4; h += 2) {
    const v4u a = w[h], b = w[h + 1];
    w[h] = v4u{__builtin_amdgcn_perm(b[0], a[0], 0x05040100u), __builtin_amdgcn_perm(b[0], a[0], 0x07060302u),
               __builtin_amdgcn_perm(b[1], a[1], 0x05040100u), __builtin_amdgcn_perm(b[1], a[1], 0x07060302u)};
    w[h + 1] = v4u{__builtin_amdgcn_perm(b[2], a[2], 0x05040100u), __builtin_amdgcn_perm(b[2], a[2], 0x07060302u),
                   __builtin_amdgcn_perm(b[3], a[3], 0x05040100u), __builtin_amdgcn_perm(b[3], a[3], 0x07060302u)};
  }
  asm volatile("" : "+v"(w[0]), "+v"(w[1]), "+v"(w[2]), "+v"(w[3]));
}
struct Acc8 { f32x2 u[4]; };
__device__ __forceinline__ Acc8 zero8() {
  Acc8 z;
  z.u[0] = z.u[1] = z.u[2] = z.u[3] = f32x2{0.f, 0.f};
  asm volatile("" : "+v"(z.u[0]), "+v"(z.u[1]), "+v"(z.u[2]), "+v"(z.u[3]));
  return z;
}
__device__ __forceinline__ f32x2 gemv_tile(unsigned x01, unsigned x23, const v4u (&w)[4], Acc8 z) {
#pragma unroll
  for (int e = 0; e < 8; ++e) z.u[e >> 1][e & 1] = dot2(w[e >> 2][e & 3], x01, z.u[e >> 1][e & 1]);
#pragma unroll
  for (int e = 0; e < 8; ++e) z.u[e >> 1][e & 1] = dot2(w[2 + (e >> 2)][e & 3], x23, z.u[e >> 1][e & 1]);
  const f32x2 r = tr16p(tr32p(z.u[0], z.u[2]), tr32p(z.u[1], z.u[3]));
  float r0 = r[0], r1 = r[1];
  r0 += dpp<ROR8>(r0); r1 += dpp<ROR8>(r1);
  r0 += dpp<ROR4>(r0); r1 += dpp<ROR4>(r1);
  PIN1(r0); PIN1(r1);
  return f32x2{r0, r1};
}
__device__ __forceinline__ unsigned gemv_col(int lane) { return (lane & 3) * 8 + (lane >> 4) * 2; }

struct PassC { unsigned sel0, sel1, mw, ms, mo, wr, rd, st, le; f32x2 m01, m23; unsigned so, sl; };
__device__ __forceinline__ PassC pass_consts(const Ctx& c, unsigned slot) {
  const int lane = fresh(0), wave = sfresh(c.wave);
  PassC a;
  const unsigned c0 = sfresh(0x0c0c0100), c1 = sfresh(0x0c0c0302);
  a.sel0 = __builtin_amdgcn_alignbit(c0, c0, lane * 16); a.sel1 = __builtin_amdgcn_alignbit(c1, c1, lane * 16);
  const int wi = (lane >> 1) & 3;
  a.m01 = f32x2{wi == 0 ? 1.f : 0.f, wi == 1 ? 1.f : 0.f}; a.m23 = f32x2{wi == 2 ? 1.f : 0.f, wi == 3 ? 1.f : 0.f};
  a.mw = L_MST + (lane & 8) * 8 + wave * 8;
  a.ms = L_MST + (lane >> 5) * 64 + (lane & 7) * 8;
  a.mo = L_MST + (lane >> 5) * 64 + wave * 8;
  a.wr = L_MRG + wave * 2048 + (lane >> 5) * 1024 + (lane & 15) * 32 + (lane & 16);
  a.rd = L_MRG + wave * 256 + lane * 4;
  a.st = (lane >> 1) * 4;
  a.le = (lane >> 5) * 4;
  a.so = slot + wave * 128;
  a.sl = wave == 0 ? slot + 1024 : WS_BYTES;
  asm volatile("" : "+v"(a.sel0), "+v"(a.sel1), "+v"(a.m01), "+v"(a.m23), "+v"(a.mw), "+v"(a.ms), "+v"(a.mo));
  asm volatile("" : "+v"(a.wr), "+v"(a.rd), "+v"(a.st), "+v"(a.le), "+s"(a.so), "+s"(a.sl));
  return a;
}
__device__ __forceinline__ void softmax16(const float (&s)[16], unsigned (&pp)[8], const PassC& a) {
  float mx = fmx(s[0], s[1]);
#pragma unroll
  for (int j = 2; j < 16; j += 2) mx = fmx(fmx(mx, s[j]), s[j + 1]);
  mx = maxx32(maxx16(mx));
  const float m = mx * SCL;
  f32x2 sum;
#pragma unroll
  for (int j = 0; j < 8; ++j) {
    const f32x2 t = fma2(f32x2{s[2 * j], s[2 * j + 1]}, sp2(SCL), sp2(-m));
    const f32x2 e = {__builtin_amdgcn_exp2f(t[0]), __builtin_amdgcn_exp2f(t[1])};
    sum = j ? sum + e : e;
    pp[j] = cvtpk(e[0], e[1]);
  }
  lds<f32x2>(a.mw) = f32x2{m, sumx32(sumx16(sum[0] + sum[1]))};
}
__device__ __forceinline__ v4u pv_frag(const unsigned (&pp)[8], int j, const PassC& a) {
  const float t = asf(__builtin_amdgcn_perm(0u, pp[j >> 1], (j & 1) ? a.sel1 : a.sel0));
  const f32x2 x = sp2(t) * a.m01, y = sp2(t) * a.m23;
  return v4u{asu(x[0]), asu(x[1]), asu(y[0]), asu(y[1])};
}
__device__ __forceinline__ float cu_scale(const Ctx& c, const PassC& a) {
  const f32x2 st = lds<f32x2>(a.ms);
  const float own = lds<float>(a.mo);
  float M = fmx(st[0], dpp<HMIR>(st[0])); M = fmx(M, dpp<XOR1>(M)); M = fmx(M, dpp<XOR2>(M));
  const float Ls = sum8(__builtin_amdgcn_exp2f(st[0] - M) * st[1]);
  const float lse = M + __builtin_amdgcn_logf(Ls);
  st4(nosent(asu(lse)), c.rws, a.le, a.sl);
  return __builtin_amdgcn_exp2f(own - lse);
}
__device__ __forceinline__ void merge_pub(const Ctx& c, const PassC& a, f32x4 a0, f32x4 a1, float f) {
  lds<f32x4>(a.wr) = a0 * f; lds<f32x4>(a.wr + 512) = a1 * f;
  wg_bar();
  f32x2 v[4];
#pragma unroll
  for (int w = 0; w < 4; ++w) v[w] = f32x2{lds<float>(a.rd + w * 4096), lds<float>(a.rd + w * 4096 + 2048)};
  const f32x2 o = (v[0] + v[1]) + (v[2] + v[3]);
  st4(nosent(pack_pair(o[0] + o[1])), c.rws, a.st, a.so);
}

__device__ __forceinline__ unsigned part_slot(int bt, int pass, int hk, int s) { return WS_PART + ((((unsigned)bt * 2 + pass) * 8 + hk) * 16 + s) * PSL; }

__device__ __forceinline__ f32x2 sum7(f32x4 a0, f32x4 a1, f32x4 a2, f32x2 a3) {
  return (((f32x2{a0[0], a0[1]} + f32x2{a0[2], a0[3]}) + (f32x2{a1[0], a1[1]} + f32x2{a1[2], a1[3]})) + ((f32x2{a2[0], a2[1]} + f32x2{a2[2], a2[3]}) + a3)) * (1.f / 448.f);
}
template <int NR>
__device__ __forceinline__ void front(const Params& p, const Ctx& c, int bt0, int pass, unsigned lb, const v4u (&wo)[4], v4u (&w)[4]) {
  if (c.s >= 14) { load_wq(c, fresh(0), w); return; }
  const int lane = fresh(0);
  const int wave = sfresh(c.wave), hk = sfresh(c.hk), cs = sfresh(c.s);
  const int pr = 7 - wave;
  const int sb = 8 + (pass ? 4 : bt0 >> 1) * 24; (void)sb;
  unsigned rr, vcb, scb;
  {
    v4u g0[NR], g1[NR]; unsigned e0[NR], e1[NR];
    const unsigned ve = (lane >> 3) * PSL, vo = ve + (lane & 7) * 16;
    const unsigned s0 = part_slot(bt0, pass, hk, 0), se = s0 + 1024 + (wave >> 2) * 4, sg = s0 + wave * 128;
    constexpr unsigned RS = 2 * 8 * 16 * PSL;
    auto ld_e = [&]() {
#pragma unroll
      for (int r = 0; r < NR; ++r) { e0[r] = ld4<AUX_SC1>(c.rws, ve, se + r * RS); e1[r] = ld4<AUX_SC1>(c.rws, ve, se + r * RS + 8 * PSL); }
    };
    auto ld_g = [&]() {
#pragma unroll
      for (int r = 0; r < NR; ++r) { g0[r] = ld16<AUX_SC1>(c.rws, vo, sg + r * RS); g1[r] = ld16<AUX_SC1>(c.rws, vo, sg + r * RS + 8 * PSL); }
    };
    auto bad_e = [&]() { unsigned mx = 0;
#pragma unroll
      for (int r = 0; r < NR; ++r) mx = max(mx, max(e0[r], e1[r]));
      return pending(mx); };
    auto bad_g = [&]() { unsigned mx = 0;
#pragma unroll
      for (int r = 0; r < NR; ++r) mx = max(mx, max(umax4(g0[r]), umax4(g1[r])));
      return pending(mx); };
    ld_e();
    SCHED();
    unsigned r1 = lb + FR_RED1 + wave * 128 + gemv_col(lane) * 4;
    rr = lb + pr * FR_ROW + FR_RED1 + (lane >> 5) * 512 + (lane & 31) * 4; vcb = (lane & 31) * 4;
    scb = WS_CB + ((bt0 + pr) * 2 + pass) * CBR + hk * 1792 + cs * 128;
    asm volatile("" : "+v"(r1), "+v"(rr), "+v"(vcb), "+s"(scb));
    Acc8 za = zero8(), zb = zero8();
    __builtin_amdgcn_s_sleep(NR == 1 ? SLP_A0 : 12);
    CFENCE();
    ld_g();
    SCHED();
    if (bad_e()) {
      _Pragma("nounroll") for (unsigned sp = 0; sp < SPIN_CAP; ++sp) { __builtin_amdgcn_s_sleep(SLP_A); CFENCE(); ld_e(); if (!bad_e()) break; }
      CFENCE();
      ld_g();
    }
    unsigned x0l[NR], x0h[NR], x1l[NR], x1h[NR];
#pragma unroll
    for (int r = 0; r < NR; ++r) {
      float M = fmx(asf(e0[r]), asf(e1[r]));
      M = fmx(M, dpp<ROR8>(M)); M = maxx32(maxx16(M));
      const unsigned xp = cvtpk(__builtin_amdgcn_exp2f(asf(e0[r]) - M), __builtin_amdgcn_exp2f(asf(e1[r]) - M));
      x0l[r] = xp & 0xffffu; x0h[r] = xp << 16; x1l[r] = xp >> 16; x1h[r] = xp & 0xffff0000u;
      float S = asf(x0h[r]) + asf(x1h[r]); S += dpp<ROR8>(S);
      e0[r] = asu(__builtin_amdgcn_rcpf(sumx32(sumx16(S))));
      if (NR == 1) asm volatile("" : "+v"(x0l[r]), "+v"(x0h[r]), "+v"(x1l[r]), "+v"(x1h[r]), "+v"(e0[r]));
    }
    if (NR == 1) SCHED();
    if (bad_g()) {
      _Pragma("nounroll") for (unsigned sp = 0; sp < SPIN_CAP; ++sp) { if (SLP_R) __builtin_amdgcn_s_sleep(SLP_R); CFENCE(); ld_g(); if (!bad_g()) break; }
    }
    STAMPF(sb + 2);
#pragma unroll
    for (int r = 0; r < NR; ++r) {
      Acc8 a = za;
#pragma unroll
      for (int e = 0; e < 4; ++e) { a.u[e][0] = dot2(g0[r][e], x0l[r], a.u[e][0]); a.u[e][1] = dot2(g0[r][e], x0h[r], a.u[e][1]); }
#pragma unroll
      for (int e = 0; e < 4; ++e) { a.u[e][0] = dot2(g1[r][e], x1l[r], a.u[e][0]); a.u[e][1] = dot2(g1[r][e], x1h[r], a.u[e][1]); }
      const f32x2 cc = tr16p(tr32p(a.u[0], a.u[2]), tr32p(a.u[1], a.u[3]));
      const int o0 = __builtin_bit_cast(int, cc[0] + dpp<ROR8>(cc[0])), o1 = __builtin_bit_cast(int, cc[1] + dpp<ROR8>(cc[1]));
      const float o = __builtin_bit_cast(float, __builtin_amdgcn_update_dpp(o0, o1, 0xE4, 0xf, 0xc, false));
      const float op = asf(cvtpk(o, dpp<XOR1>(o)));
      lds<f32x2>(r1 + r * FR_ROW) = gemv_tile(asu(dpp<0x00>(op)), asu(dpp<0xAA>(op)), wo, zb) * asf(e0[r]);
    }
  }
  STAMPF(sb + 3);
  wg_bar();
  if (pr < NR) {
    const f32x2 a = f32x2{lds<float>(rr), lds<float>(rr + 128)} + f32x2{lds<float>(rr + 256), lds<float>(rr + 384)};
    st4(nosent(asu(sumx32(a[0] + a[1]))), c.rws, vcb, scb);
  }
  STAMPF(sb + 4);
  if (NR == 1) { lds<v4u>(L_MRG + wave * 2048 + lane * 16) = wo[2]; lds<v4u>(L_MRG + wave * 2048 + lane * 16 + 1024) = wo[3]; }
  const unsigned cidx = hk * 14 + cs;
  auto finish = [&](auto&& last_bar) {
    unsigned fb = lb + pr * FR_ROW + (lane & 3) * 4 + FR_RED2, vxd = ((lane >> 2) & (XD_REP - 1)) * 1792 + (lane & 3) * 4;
    unsigned sxd = WS_XD + (bt0 + pr) * XD_REP * 1792 + cidx * 16;
    float xb = lds<float>(lb + pr * FR_ROW + (lane & 3) * 4 + FR_X1 + cidx * 16) + bflo(*(const unsigned short*)p.b2);
    unsigned* op = (unsigned*)((char*)p.out + (bt0 + pr) * 896 + cidx * 8 + (lane & 2) * 2);
    asm volatile("" : "+v"(fb), "+v"(vxd), "+s"(sxd), "+v"(xb), "+v"(op));
    last_bar();
    f32x2 a[3];
#pragma unroll
    for (int ww = 0; ww < 3; ++ww) a[ww] = f32x2{lds<float>(fb + ww * 32), lds<float>(fb + ww * 32 + 16)};
    const f32x2 t = (a[0] + a[1]) + (a[2] + f32x2{lds<float>(fb + 96), xb});
    const float x2 = t[0] + t[1];
    if (pass == 0) st4(nosent(asu(x2)), c.rws, vxd, sxd);
    else *(__attribute__((address_space(1))) unsigned*)op = pack_pair(x2);
  };
  if (wave < 7) {
    const int t4 = (wave * 64 + lane) * 8;
    const v2u pw1 = *(const v2u*)((const char*)p.w1 + t4), pb1 = *(const v2u*)((const char*)p.b1 + t4), pw2 = *(const v2u*)((const char*)p.w2 + t4);
    v4u g[NR][2]; unsigned rs[NR];
    const unsigned vo = (lane >> 4) * (2 * 1792) + (lane & 15) * 16;
    const unsigned dl = (lane & 15) * 16 + (lane >> 4) * 4;
    const unsigned so = WS_CB + (bt0 * 2 + pass) * CBR + wave * 256;
    const unsigned sr = pass ? WS_XD + (bt0 * XD_REP + (hk & (XD_REP - 1))) * 1792 : WS_X0 + bt0 * 1792, srs = pass ? XD_REP * 1792 : 1792;
    auto ld_r = [&]() {
#pragma unroll
      for (int r = 0; r < NR; ++r) rs[r] = ld4<AUX_SC1>(c.rws, dl, sr + r * srs + wave * 256);
    };
    auto ld_g = [&]() {
#pragma unroll
      for (int r = 0; r < NR; ++r) { g[r][0] = ld16<AUX_SC1>(c.rws, vo, so + r * 2 * CBR); g[r][1] = ld16<AUX_SC1>(c.rws, vo + 1792, so + r * 2 * CBR); }
    };
    auto bad = [&]() { unsigned mx = 0;
#pragma unroll
      for (int r = 0; r < NR; ++r) mx = max(max(mx, rs[r]), max(umax4(g[r][0]), umax4(g[r][1])));
      return pending(mx); };
    ld_r();
    unsigned xw = lb + FR_X1 + wave * 256 + dl, sw = lb + FR_ST + ((lane & 16) ? wave * 8 + (lane >> 5) * 4 : 56);
    unsigned zw = lb + FR_RED2 + wave * 16 + (lane >> 4) * 4, sa = lb + FR_ST, xa = lb + FR_X1 + cidx * 16, wqo = wq_off(lane);
    asm volatile("" : "+v"(xw), "+v"(sw), "+v"(zw), "+v"(sa), "+v"(xa), "+v"(wqo));
    __builtin_amdgcn_s_sleep(NR == 1 ? SLP_B : 24);
    CFENCE();
    ld_g();
    SCHED();
    f32x2 w1f[2] = {bf2(pw1[0]), bf2(pw1[1])}, b1f[2] = {bf2(pb1[0]), bf2(pb1[1])}, w2f[2] = {bf2(pw2[0]), bf2(pw2[1])};
    asm volatile("" : "+v"(w1f[0]), "+v"(w1f[1]), "+v"(b1f[0]), "+v"(b1f[1]), "+v"(w2f[0]), "+v"(w2f[1]));
    SCHED();
    if (bad()) {
      _Pragma("nounroll") for (unsigned sp = 0; sp < SPIN_CAP; ++sp) { if (SLP_RB) __builtin_amdgcn_s_sleep(SLP_RB); CFENCE(); ld_r(); ld_g(); if (!bad()) break; }
    }
    STAMPF(sb + 5);
#pragma unroll
    for (int r = 0; r < NR; ++r) {
      const f32x4 gs = __builtin_bit_cast(f32x4, g[r][0]) + __builtin_bit_cast(f32x4, g[r][1]);
      const f32x2 t = tr32p(f32x2{gs[0], gs[1]}, f32x2{gs[2], gs[3]});
      const float x1 = asf(rs[r]) + tr16(t[0], t[1]);
      lds<float>(xw + r * FR_ROW) = x1;
      float sv = sum16(tr32(x1, x1 * x1));
      sv += dpp<0x142>(sv);
      lds<float>(sw + r * FR_ROW) = sv;
    }
    STAMPF(sb + 6);
    wg_bar();
#pragma unroll
    for (int r = 0; r < NR; ++r) {
      const f32x4 a0 = lds<f32x4>(sa + r * FR_ROW), a1 = lds<f32x4>(sa + r * FR_ROW + 16), a2 = lds<f32x4>(sa + r * FR_ROW + 32);
      const f32x2 a3 = lds<f32x2>(sa + r * FR_ROW + 48);
      const f32x4 x1 = lds<f32x4>(xa + r * FR_ROW);
      if (NR == 1) { load_wq2(c, wqo, w, 0); SCHED(); }
      const f32x2 t = sum7(a0, a1, a2, a3);
      const float var = fmx(fmaf(-t[0], t[0], t[1]), 0.f);
      const f32x4 hv4 = (x1 - t[0]) * __builtin_amdgcn_rsqf(fmaf(var, 1.f + EPS, EPS * EPS));
      float y[4];
#pragma unroll
      for (int j = 0; j < 4; ++j) {
        f32x2 acc;
#pragma unroll
        for (int f = 0; f < 2; ++f) {
          const f32x2 u = fma2(sp2(hv4[j]), w1f[f], b1f[f]);
          const f32x2 tt = u * fma2(u * u, sp2(-0.10294324f), sp2(-2.3022082f));
          const f32x2 d = f32x2{__builtin_amdgcn_exp2f(tt[0]), __builtin_amdgcn_exp2f(tt[1])} + 1.f;
          const f32x2 sg = {__builtin_amdgcn_rcpf(d[0]), __builtin_amdgcn_rcpf(d[1])};
          acc = f ? fma2(u * w2f[f], sg, acc) : (u * w2f[f]) * sg;
        }
        y[j] = acc[0] + acc[1];
        if (NR == 1 && j == 1) { PIN2(y[0], y[1]); SCHED(); load_wq2(c, wqo, w, 2); SCHED(); }
      }
      const f32x2 z = tr32p(f32x2{y[0], y[1]}, f32x2{y[2], y[3]});
      lds<float>(zw + r * FR_ROW) = sum16(tr16(z[0], z[1]));
    }
    STAMPF(sb + 7);
    if (NR > 1 && pr < NR) finish(wg_bar);
    else wg_bar();
  } else {
    wg_bar();
    finish(wg_bar);
    if (NR == 1) asm volatile("" : "=v"(w[0]), "=v"(w[1]), "=v"(w[2]), "=v"(w[3]));
  }
}

__device__ __forceinline__ void qproj1(const float (&d)[4], const v4u (&w)[4], unsigned ua, unsigned sa, Acc8 z) {
  const f32x2 y = gemv_tile(cvtpk(d[0], d[1]), cvtpk(d[2], d[3]), w, z);
  float s = tr32((d[0] + d[1]) + (d[2] + d[3]), fmaf(d[0], d[0], d[1] * d[1]) + fmaf(d[2], d[2], d[3] * d[3]));
  s = sumx16(s); s += dpp<ROR8>(s); s += dpp<ROR4>(s);
  lds<f32x2>(ua) = y;
  lds<float>(sa) = s;
}
__device__ __forceinline__ unsigned qp_ua(int lane, int wave) { return wave * 128 + gemv_col(lane) * 4; }
__device__ __forceinline__ unsigned qp_sa(int lane, int wave) { return 1024 + wave * 8 + (lane >> 5) * 4; }
__device__ __forceinline__ void qproj2(const Ctx& c, unsigned lb, unsigned ra, unsigned vo, unsigned so, float cs) {
  const f32x4 a0 = lds<f32x4>(lb + 1024), a1 = lds<f32x4>(lb + 1040), a2 = lds<f32x4>(lb + 1056);
  const f32x2 a3 = lds<f32x2>(lb + 1072);
  const f32x2 u = f32x2{lds<float>(ra), lds<float>(ra + 128)} + f32x2{lds<float>(ra + 256), lds<float>(ra + 384)};
  const float U = sumx32(u[0] + u[1]);
  const f32x2 t = sum7(a0, a1, a2, a3);
  const float var = fmx(fmaf(-t[0], t[0], t[1]), 0.f);
  const float q = fmaf(-t[0], cs, U) * __builtin_amdgcn_rsqf(var + EPS);
  st4(nosent(pack_pair(q)), c.rws, vo, so);
}
__device__ __forceinline__ void colsum1(int lane, int wave, const v4u (&w)[4]) {
  const f32x2 y = gemv_tile(0x3f803f80u, 0x3f803f80u, w, zero8());
  lds<f32x2>(L_BK + qp_ua(lane, wave)) = wave < 7 ? y : f32x2{0.f, 0.f};
}
__device__ __forceinline__ float colsum2(int lane) {
  float a = 0.f;
#pragma unroll
  for (int ww = 0; ww < 7; ++ww) a += lds<float>(L_BK + ww * 128 + (lane & 31) * 4);
  return a;
}
__device__ __forceinline__ void wait_flags(const Ctx& c, int lane, unsigned so, u64 tag) {
  _Pragma("nounroll") for (unsigned sp = 0; sp < SPIN_CAP; ++sp) {
    CFENCE();
    const v4u f = ld16<AUX_SC1>(c.rws, lane * 16, so);
    if (__builtin_amdgcn_ballot_w64(!(f[0] == (unsigned)tag && f[1] == (unsigned)(tag >> 32) && f[2] == (unsigned)tag && f[3] == (unsigned)(tag >> 32))) == 0) break;
    __builtin_amdgcn_s_sleep(24);
  }
  CFENCE();
}
__device__ __forceinline__ void init_markers(const Ctx& c, int cc, int tid) {
  const v4u sent = {SENT, SENT, SENT, SENT};
  if (c.wave >= 2)
    for (unsigned o = WS_S0 + (cc * 384 + tid - 128) * 16; o < WS_SEND; o += 128 * 384 * 16) st16(sent, c.rws, o, 0);
}
__device__ __forceinline__ void wait_token(const Ctx& c, int bt, u64 tag) {
  _Pragma("nounroll") for (unsigned sp = 0; sp < SPIN_CAP; ++sp) {
    CFENCE();
    const v2u t = ld8<AUX_SC1>(c.rws, 0, WS_TOK + bt * 1024 + c.hk * 128);
    if ((unsigned)__builtin_amdgcn_readfirstlane(t[0]) == (unsigned)tag && (unsigned)__builtin_amdgcn_readfirstlane(t[1]) == (unsigned)(tag >> 32)) break;
    __builtin_amdgcn_s_sleep(4);
  }
}
__device__ __forceinline__ void fetch_q1(const Ctx& c, int lane, unsigned so, u64 tag) {
  wait_flags(c, lane, WS_FLAGS, tag);
  lds<v4u>(L_Q + lane * 16) = ld16<AUX_SC1>(c.rws, lane * 16, so);
  asm volatile("s_waitcnt lgkmcnt(0)" ::: "memory");
  *(volatile LAS u64*)(size_t)L_QF = tag;
}

__global__ void __launch_bounds__(512, 2) fused_decode_kernel(Params p) {
  extern __shared__ __attribute__((aligned(16))) char smem[];
  asm volatile("" :: "s"((unsigned)(size_t)(LAS char*)smem));
  Ctx c;
  const int tid = threadIdx.x;
  int lane = tid & 63;
  c.wave = __builtin_amdgcn_readfirstlane(tid >> 6);
  const int blk = blockIdx.x;
  const int grp = (blk >> 5) & 1, cc = (blk & 31) | ((blk >> 6) << 5);
  c.hk = cc & 7; c.s = cc >> 3;
  c.rws = mkrs(p.ws, WS_BYTES); c.rwo = mkrs(p.wo, 4096 * 448 * 2); c.rwq = mkrs(p.wq, 4096 * 448 * 2);
  const rsrc_t rk = mkrs(p.k, 1u << 28), rv = mkrs(p.v, 1u << 28);
  const u64 t_start = __builtin_amdgcn_s_memrealtime();
  STAMP_INIT();
  STAMP(0);

  const v2u ep = ld8<AUX_SC1>(c.rws, 0, WS_EPOCH);
  unsigned tx = 0;
  {
    constexpr unsigned xs[4] = XS, xh[3] = XH, xw[3] = XW;
#pragma unroll
    for (int i = 0; i < 4; ++i) tx ^= ((c.s >> i) & 1) * xs[i];
#pragma unroll
    for (int i = 0; i < 3; ++i) tx ^= ((c.hk >> i) & 1) * xh[i] ^ ((c.wave >> i) & 1) * xw[i];
  }
  const unsigned kvw = sfresh((c.hk * 8192 + c.s * 512 + c.wave * 64) * 512 + tx * 2048);
#define KV_SETUP() const int lk_ = fresh(lane); const unsigned ko = ((lk_ >> 2) & 3) * 512 + (lk_ >> 4) * 64 + (lk_ & 3) * 16, vo = (lk_ >> 4) * 512 + (lk_ & 15) * 16, \
    kl = L_K + c.wave * 16384 + lk_ * 16; (void)kl; (void)ko; (void)vo
#define K_LD(base, j) ld16<AUX_NT>(rk, ko + ((j) & 1) * 256, (base) ^ (((j) >> 1) * 2048))
#define V_LD(base, j) ld16<AUX_NT>(rv, vo + ((j) & 1) * 256, (base) ^ (((j) >> 1) * 2048))
#define WIN_WAIT() do { if (LD_WIN) { SCHED(); asm volatile("s_waitcnt vmcnt(%0)" :: "i"(LD_WIN)); SCHED(); } } while (0)
#define NX_LD(base, n) do { if ((n) < 16) K[(n) & 15] = K_LD(base, n); else if ((n) < 32) V[((n) - 16) & 31] = K_LD(base, n); else V[((n) - 16) & 31] = V_LD(base, (n) - 16); } while (0)
#define PV_STEP(j) do { const v4u A_ = pv_frag(pp, j, pc); a0 = mfma(A_, V[2 * (j)], a0); a1 = mfma(A_, V[2 * (j) + 1], a1); } while (0)

  v4u K[16], V[32], wo[4], q[2];
  u64 tag;
  int bt = grp;
  {
    if (grp == 1) {
      u64 pc;
      asm volatile("s_getpc_b64 %0" : "=s"(pc));
      const unsigned pf = *(const __attribute__((address_space(1))) unsigned*)((pc & ~255ull) + (c.s * 6 + (c.wave < 6 ? c.wave : 0)) * 256 + lane * 4);
      v4u wq[4];
      load_wq(c, lane, wq);
      const int xo = ((c.wave < 7 ? c.wave : 0) * 64 + (lane >> 2) * 4) * 2;
      v2u xr[8];
#pragma unroll
      for (int b = 0; b < 8; ++b) xr[b] = *(const v2u*)((const char*)p.x + b * 896 + xo);
      const unsigned xc = ((const unsigned short*)p.x)[(cc & 7) * 448 + (tid < 448 ? tid : 0)];
      STAMP(3);
      pair_rows(wq);
      asm volatile("" :: "v"(pf));
      STAMP(4);
      colsum1(lane, c.wave, wq);
      const u64 tg = mktag(ep);
      float cs = 0.f;
#pragma unroll
      for (int ph = 0; ph < 2; ++ph) {
        if (ph == 1) { load_wo(c, lane, wo); init_markers(c, cc, tid); }
        if (ph == 1 && cc < 8 && tid < 448) st4(nosent(xc << 16), c.rws, tid * 4, WS_X0 + cc * 1792);
#pragma unroll
        for (int b = 2 * ph; b < (ph ? 8 : 2); ++b) {
          if (c.wave < 7) {
            const float d[4] = {bflo(xr[b][0]), bfhi(xr[b][0]), bflo(xr[b][1]), bfhi(xr[b][1])};
            qproj1(d, wq, L_K + b * BK_ROW + qp_ua(lane, c.wave), L_K + b * BK_ROW + qp_sa(lane, c.wave), zero8());
          } else {
            lds<float>(L_K + b * BK_ROW + 7 * 128 + (lane & 31) * 4) = 0.f;
          }
        }
        wg_bar();
        if (ph == 0) { cs = colsum2(lane); if (c.wave == 0) lds<float>(L_CS + (lane & 31) * 4) = cs; }
        if ((c.wave >= 2) == (ph == 1))
          qproj2(c, L_K + c.wave * BK_ROW, L_K + c.wave * BK_ROW + (lane >> 5) * 512 + (lane & 31) * 4, ((lane & 31) >> 1) * 4, WS_Q1 + (c.wave * 8 + c.hk) * 1024 + c.s * 64, cs);
        if (ph == 1 || c.wave < 2) asm volatile("s_waitcnt vmcnt(0)" ::: "memory");
        wg_bar();
        if (tid == 0) st8(tg, c.rws, cc * 8, WS_FLAGS + ph * 1024);
        STAMP(ph ? 5 : 1);
      }
      pair_rows(wo);
      if (c.wave == 0) {
        fetch_q1(c, lane, WS_Q1 + (8 + c.hk) * 1024, tg);
        wait_token(c, 0, tg);
      }
      wg_bar();
    } else {
      wo[0] = wo[1] = wo[2] = wo[3] = v4u{0u, 0u, 0u, 0u};
    }
    const unsigned base = bt * (32u << 20) + kvw;
    KV_SETUP();
    SCHED();
#pragma unroll
    for (int n = 0; n < 48; ++n) {
      if (c.wave != 0 || grp != 0) WIN_WAIT();
      NX_LD(base, n);
    }
    SCHED();
    STAMP(6);
    tag = mktag(ep);
    if (grp == 0 && c.wave == 0) {
      while (__builtin_amdgcn_s_memrealtime() - t_start < FIRST_POLL_TICKS) __builtin_amdgcn_s_sleep(4);
      fetch_q1(c, lane, WS_Q1 + c.hk * 1024, tag);
    }
    _Pragma("nounroll") for (unsigned sp = 0; sp < 64 * SPIN_CAP; ++sp) {
      CFENCE();
      if (*(volatile LAS u64*)(size_t)L_QF == tag) break;
      __builtin_amdgcn_s_sleep(2);
    }
    CFENCE();
    STAMP(7);
    q[0] = lds<v4u>(L_Q + q_off(lane));
    q[1] = lds<v4u>(L_Q + q_off(lane) + 256);
  }

#pragma unroll 1
  for (int it = 0;; ++it) {
    const bool fin = it == 4;
    if (!fin) {
      const unsigned base = bt * (32u << 20) + kvw;
      const int wave = sfresh(c.wave);
      float s[16];
      unsigned pp[8];
      PassC pc;
      {
        KV_SETUP();
#pragma unroll
        for (int j = 0; j < 8; ++j) {
          const f32x4 d0 = qk_mm(V[2 * j], V[2 * j + 1], q), d1 = qk_mm(K[2 * j], K[2 * j + 1], q);
          lds<v4u>(kl + (2 * j) * 1024) = V[2 * j]; lds<v4u>(kl + (2 * j + 1) * 1024) = V[2 * j + 1];
          s[8 + j] = qk_red(d0); s[j] = qk_red(d1);
          PIN2(s[j], s[8 + j]);
          SCHED();
          if (j == TOK_J) st8(((u64)(unsigned)sfresh(tag >> 32) << 32) | (unsigned)sfresh(tag), c.rws, (lk_ & 7) * 128, cc == TOK_CU && wave == 7 ? WS_TOK + bt * 1024 : WS_BYTES);
          if (P1_MODE == 0) V[j] = V_LD(base, j);
          else if (j < 6) { V[2 * j] = V_LD(base, 2 * j); V[2 * j + 1] = V_LD(base, 2 * j + 1); }
          SCHED();
        }
        pc = pass_consts(c, part_slot(bt, 0, c.hk, c.s));
        softmax16(s, pp, pc);
        if (it == 0 && wave == 0) wait_flags(c, fresh(lane), WS_FLAGS + 1024, tag);
      }
      {
        KV_SETUP();
        SCHED();
#pragma unroll
        for (int j = (P1_MODE ? 12 : 8); j < P1_NB; ++j) V[j] = V_LD(base, j);
        wg_bar();
#pragma unroll
        for (int j = P1_NB; j < 16; ++j) V[j] = V_LD(base, j);
        SCHED();
      }
      float f = cu_scale(c, pc);
      f32x4 a0 = {0.f, 0.f, 0.f, 0.f}, a1 = {0.f, 0.f, 0.f, 0.f};
#pragma unroll
      for (int j = 8; j < 16; ++j) PV_STEP(j);
      PIN1(f);
#pragma unroll
      for (int j = 0; j < 8; ++j) PV_STEP(j);
      STAMP(8 + it * 24 + 0);
      merge_pub(c, pc, a0, a1, f);
      STAMP(8 + it * 24 + 1);
    }
    if (it == 0 && grp == 0) { load_wo(c, fresh(lane), wo); pair_rows(wo); }
    v4u wq[4];
    front<1>(p, c, bt, fin ? 1 : 0, L_FR, wo, wq);
    asm volatile("s_cmp_eq_u32 %0, 4\n\ts_cbranch_scc0 .Lgo_on%=\n\ts_endpgm\n.Lgo_on%=:" :: "s"(it) : "scc", "memory");
    STAMPF(8 + it * 24 + 8);
    const bool nxt = it < 3;
    {
      const int ln = fresh(lane), wave = sfresh(c.wave);
      pair_rows(wq);
      if (it == 0 && grp == 0) {
        colsum1(ln, wave, wq);
        wg_bar();
        if (wave == 7) lds<float>(L_CS + (ln & 31) * 4) = colsum2(ln);
        wg_bar();
      }
      unsigned sq = WS_Q2 + (bt * 8 + c.hk) * 1024, qo = q_off(ln);
      if (wave < 7) {
        unsigned ua = L_BK + qp_ua(ln, wave), sa = L_BK + qp_sa(ln, wave), xo = (ln >> 2) * 16;
        unsigned sx = WS_XD + (bt * XD_REP + (c.hk % XD_REP)) * 1792 + wave * 256;
        asm volatile("" : "+v"(ua), "+v"(sa), "+v"(xo), "+s"(sx), "+v"(qo), "+s"(sq));
        const Acc8 z = zero8();
        __builtin_amdgcn_s_sleep(SLP_D);
        CFENCE();
        v4u xv = ld16<AUX_SC1>(c.rws, xo, sx);
        if (pending(umax4(xv))) {
          _Pragma("nounroll") for (unsigned sp = 0; sp < SPIN_CAP; ++sp) {
            if (c.s >= 14) __builtin_amdgcn_s_sleep(SLP_RD);
            CFENCE();
            xv = ld16<AUX_SC1>(c.rws, xo, sx);
            if (!pending(umax4(xv))) break;
          }
        }
        STAMPF(8 + it * 24 + 9);
        const float d[4] = {asf(xv[0]), asf(xv[1]), asf(xv[2]), asf(xv[3])};
        qproj1(d, wq, ua, sa, z);
        STAMPF(8 + it * 24 + 10);
        wg_bar();
        __builtin_amdgcn_s_sleep(SLP_C + 2);
      } else {
        unsigned ra = L_BK + (ln >> 5) * 512 + (ln & 31) * 4, vq = ((ln & 31) >> 1) * 4, sp2_ = sq + c.s * 64;
        float cs = lds<float>(L_CS + (ln & 31) * 4);
        asm volatile("" : "+v"(ra), "+v"(vq), "+s"(sp2_), "+v"(cs), "+v"(qo), "+s"(sq));
        wg_bar();
        qproj2(c, L_BK, ra, vq, sp2_, cs);
        STAMPF(8 + it * 24 + 11);
        __builtin_amdgcn_s_sleep(SLP_C);
      }
      CFENCE();
      q[0] = ld16<AUX_SC1>(c.rws, qo, sq); q[1] = ld16<AUX_SC1>(c.rws, qo + 256, sq);
      SCHED();
      unsigned base = (nxt ? bt + 2 : bt) * (32u << 20) + kvw, stk = WS_TOK + ((bt + 1) & 7) * 1024 + c.hk * 128;
      int gate = __builtin_amdgcn_readfirstlane(nxt ? 1 : 0);
      unsigned ko = ((ln >> 2) & 3) * 512 + (ln >> 4) * 64 + (ln & 3) * 16, kl = L_K + wave * 16384 + ln * 16, pk = L_MRG + wave * 2048 + ln * 16;
      const PassC pc = pass_consts(c, part_slot(bt, 1, c.hk, c.s));
      v4u kb[16];
#pragma unroll
      for (int u = 0; u < 4; ++u) kb[u] = lds<v4u>(kl + u * 1024);
      asm volatile("" : "+s"(base), "+s"(gate), "+s"(stk));
      asm volatile("" : "+v"(ko), "+v"(kl), "+v"(pk), "+v"(kb[0]), "+v"(kb[1]), "+v"(kb[2]), "+v"(kb[3]));
      SCHED();
      if (pending(max(umax4(q[0]), umax4(q[1])))) {
        _Pragma("nounroll") for (unsigned sp = 0; sp < SPIN_CAP; ++sp) {
          CFENCE();
          q[0] = ld16<AUX_SC1>(c.rws, qo, sq); q[1] = ld16<AUX_SC1>(c.rws, qo + 256, sq);
          if (!pending(max(umax4(q[0]), umax4(q[1])))) break;
        }
      }
      STAMP(8 + it * 24 + 12);
      float s[16];
      {
        f32x4 d[8];
#pragma unroll
        for (int j = 0; j < 8; ++j) d[j] = mfma(K[2 * j], q[0], f32x4{0.f, 0.f, 0.f, 0.f});
#pragma unroll
        for (int j = 0; j < 8; ++j) d[j] = mfma(K[2 * j + 1], q[1], d[j]);
        asm volatile("" : "+v"(d[0]), "+v"(d[1]), "+v"(d[2]), "+v"(d[3]), "+v"(d[4]), "+v"(d[5]), "+v"(d[6]), "+v"(d[7]));
        SCHED();
#pragma unroll
        for (int u = 4; u < 12; ++u) kb[u] = lds<v4u>(kl + u * 1024);
        SCHED();
        f32x4 e[8];
        e[0] = qk_mm(kb[0], kb[1], q); e[1] = qk_mm(kb[2], kb[3], q);
#pragma unroll
        for (int j = 0; j < 4; ++j) s[j] = qk_red(d[j]);
        asm volatile("" : "+v"(s[0]), "+v"(s[1]), "+v"(s[2]), "+v"(s[3]), "+v"(e[0]), "+v"(e[1]));
        SCHED();
#pragma unroll
        for (int u = 12; u < 16; ++u) kb[u] = lds<v4u>(kl + u * 1024);
        SCHED();
#pragma unroll
        for (int j = 2; j < 8; ++j) e[j] = qk_mm(kb[2 * j], kb[2 * j + 1], q);
#pragma unroll
        for (int j = 4; j < 8; ++j) s[j] = qk_red(d[j]);
#pragma unroll
        for (int j = 0; j < 8; ++j) s[8 + j] = qk_red(e[j]);
        asm volatile("" : "+v"(s[4]), "+v"(s[5]), "+v"(s[6]), "+v"(s[7]), "+v"(s[8]), "+v"(s[9]), "+v"(s[10]), "+v"(s[11]), "+v"(s[12]), "+v"(s[13]), "+v"(s[14]), "+v"(s[15]));
        SKIP_BEGIN(gate, 0, rk, ko);
#pragma unroll
        for (int n = 0; n < P2_N1; ++n) K[n] = K_LD(base, n);
        SKIP_END(0);
      }
      STAMPF(8 + it * 24 + 13);
      wo[2] = lds<v4u>(pk); wo[3] = lds<v4u>(pk + 1024);
      unsigned pp[8];
      softmax16(s, pp, pc);
      f32x4 a0 = {0.f, 0.f, 0.f, 0.f}, a1 = {0.f, 0.f, 0.f, 0.f};
#pragma unroll
      for (int j = 0; j < 8; ++j) PV_STEP(j);
      PIN2(a0, a1);
      SKIP_BEGIN(gate, 1, rk, ko);
#pragma unroll
      for (int n = P2_N1; n < P2_N2; ++n) K[n] = K_LD(base, n);
      SKIP_END(1);
      wg_bar();
      const v2u tk = ld8<AUX_SC1>(c.rws, 0, stk);
      float f = cu_scale(c, pc);
#pragma unroll
      for (int j = 8; j < 16; ++j) PV_STEP(j);
      STAMP(8 + it * 24 + 15);
      merge_pub(c, pc, a0, a1, f);
      SCHED();
      const int go = sfresh((__builtin_amdgcn_readfirstlane(tk[0]) ^ (int)tag) | (__builtin_amdgcn_readfirstlane(tk[1]) ^ (int)(tag >> 32)));
      STAMP(8 + it * 24 + 16);
      if (nxt && go != 0) wait_token(c, bt + 1, tag);
      STAMP(8 + it * 24 + 14);
      {
        KV_SETUP();
        unsigned sq1 = WS_Q1 + (((bt + 2) & 7) * 8 + c.hk) * 1024, vq1 = q_off(lk_);
        asm volatile("" : "+s"(sq1), "+v"(vq1) : "s"(rv), "v"(vo), "s"(c.rws));
        SKIP_BEGIN(gate, 4, rk, ko);
        q[0] = ld16<AUX_SC1>(c.rws, vq1, sq1); q[1] = ld16<AUX_SC1>(c.rws, vq1 + 256, sq1);
#pragma unroll
        for (int n = P2_N2; n < 48; ++n) {
          WIN_WAIT();
          NX_LD(base, n);
        }
        SKIP_END(4);
      }
      STAMP(8 + it * 24 + 17);
    }
    if (nxt) bt += 2;
    else if (grp == 0) break;
  }
  v4u wq[4];
  front<7>(p, c, 0, 1, L_K, wo, wq);
  if (cc == 0 && c.wave == 0) st8(tag, c.rws, 0, WS_EPOCH);
  STAMP_DUMP();
}

}

extern "C" void kernel_launch(void* const* d_in, const int* in_sizes, int n_in, void* d_out, int out_size, void* d_ws, size_t ws_size,
                              hipStream_t stream) {
  (void)in_sizes; (void)n_in; (void)out_size; (void)ws_size;
  Params p;
  p.x = d_in[0]; p.k = d_in[1]; p.v = d_in[2]; p.wq = d_in[3]; p.wo = d_in[4];
  p.w1 = d_in[6]; p.b1 = d_in[7]; p.w2 = d_in[8]; p.b2 = d_in[9];
  p.out = d_out;
  p.ws = d_ws;
  (void)hipFuncSetAttribute((const void*)fused_decode_kernel, hipFuncAttributeMaxDynamicSharedMemorySize, LDS_BYTES);
  hipLaunchKernelGGL(fused_decode_kernel, dim3(256), dim3(512), LDS_BYTES, stream, p);
}
```
